```python
import torch

def _softmax(x, axis=-1):
    return torch.softmax(x, dim=axis)
def _gelu(x):
    return torch.nn.functional.gelu(x, approximate="tanh")
def _silu(x):
    return torch.nn.functional.silu(x)
def _sigmoid(x):
    return torch.sigmoid(x)
def _layernorm(x, axis=-1, eps=1e-5):
    m = x.mean(dim=axis, keepdim=True)
    v = x.var(dim=axis, keepdim=True, unbiased=False)
    return (x - m) / torch.sqrt(v + eps)
def _scatter_add(tgt, idx, src, axis=0):
    return tgt.clone().index_add_(axis, idx, src)
def _segment_sum(src, seg, n):
    out = torch.zeros((n,) + src.shape[1:], dtype=src.dtype)
    return out.index_add_(0, seg, src)
def _pad(x, pw):
    flat = tuple(v for pair in reversed(pw) for v in pair)
    return torch.nn.functional.pad(x, flat)

kernel_name = 'interleave_layers__mha_block_cached__glu_gated_glu__decode'

def setup_inputs():

    _w = (lambda a: (lambda t: t.to(torch.bfloat16) if t.is_floating_point() else t)(torch.as_tensor(a)))
    def _A_setup():
        B, Sq, D = 2, 1, 448
        H, Sk, Dh = 16, 16384, 256
        return {
            "x": _w((lambda *s: torch.randn(*s, dtype=torch.bfloat16) * 0.05)(B, Sq, D)),
            "k_cache": _w((lambda *s: torch.randn(*s, dtype=torch.bfloat16) * 1.6)(B, H, Sk, Dh)),
            "v_cache": _w((lambda *s: torch.randn(*s, dtype=torch.bfloat16) * 1.6)(B, H, Sk, Dh)),
            "wq": _w((lambda *s: torch.randn(*s, dtype=torch.bfloat16) * 1.6)(D, H * Dh) * (D ** -0.5)),
            "wo": _w((lambda *s: torch.randn(*s, dtype=torch.bfloat16) * 1.6)(H * Dh, D) * ((H * Dh) ** -0.5)),
        }
    _A_in = _A_setup()

    def _B_setup():
        B, S, D, F = 2, 1, 448, 1792
        return {
            "x": _w((lambda *s: torch.randn(*s, dtype=torch.bfloat16) * 0.05)(B, S, D)),
            "w_gate": _w((lambda *s: torch.randn(*s, dtype=torch.bfloat16) * 0.05)(D, F)),
            "w_up": _w((lambda *s: torch.randn(*s, dtype=torch.bfloat16) * 0.05)(D, F)),
            "w_down": _w((lambda *s: torch.randn(*s, dtype=torch.bfloat16) * 0.05)(F, D)),
        }
    _B_in = _B_setup()

    return {
        "A_x": _A_in["x"],
        "A_k_cache": _A_in["k_cache"],
        "A_v_cache": _A_in["v_cache"],
        "A_wq": _A_in["wq"],
        "A_wo": _A_in["wo"],
        "B_x": _B_in["x"],
        "B_w_gate": _B_in["w_gate"],
        "B_w_up": _B_in["w_up"],
        "B_w_down": _B_in["w_down"],
    }

def reference(A_x, A_k_cache, A_v_cache, A_wq, A_wo, B_x, B_w_gate, B_w_up, B_w_down):
    def _A_ref(x, k_cache, v_cache, wq, wo):
        B, Sq, D = x.shape
        H, Dh = 16, 256
        q = (lambda x, *d: x.permute(*d))((x @ wq).reshape(B, Sq, H, Dh), 0, 2, 1, 3)
        scores = torch.einsum("bhqd,bhkd->bhqk", q, k_cache) * (Dh ** -0.5)
        p = _softmax(scores, axis=-1)
        o = torch.einsum("bhqk,bhkd->bhqd", p, v_cache)
        return (lambda x, *d: x.permute(*d))(o, 0, 2, 1, 3).reshape(B, Sq, H * Dh) @ wo

    def _B_ref(x, w_gate, w_up, w_down):
        g = torch.einsum("bsd,df->bsf", x, w_gate)
        u = torch.einsum("bsd,df->bsf", x, w_up)
        h = _sigmoid(g) * u
        return torch.einsum("bsf,fd->bsd", h, w_down)

    x = A_x
    x = x + _A_ref(_layernorm(x, axis=-1), A_k_cache, A_v_cache, A_wq, A_wo)
    x = x + _B_ref(_layernorm(x, axis=-1), B_w_gate, B_w_up, B_w_down)
    x = x + _A_ref(_layernorm(x, axis=-1), A_k_cache, A_v_cache, A_wq, A_wo)
    x = x + _B_ref(_layernorm(x, axis=-1), B_w_gate, B_w_up, B_w_down)
    return x
```

```cpp
#include <hip/hip_runtime.h>
#include <hip/hip_bf16.h>
#define POL 1
typedef unsigned u32;
typedef u32 u32x4 __attribute__((ext_vector_type(4)));
typedef __bf16 bf16x2 __attribute__((ext_vector_type(2)));
typedef unsigned short u16;
#define DEV __device__ __forceinline__
DEV float bf2f(u16 v){ return __uint_as_float((u32)v<<16); }
DEV float dot2(u32 a,u32 b,float c){ return __builtin_amdgcn_fdot2_f32_bf16(__builtin_bit_cast(bf16x2,a),__builtin_bit_cast(bf16x2,b),c,false); }
DEV u32 pk2(float a,float b){ bf16x2 v; v.x=(__bf16)a; v.y=(__bf16)b; return __builtin_bit_cast(u32,v); }
DEV u32x4 ldg(const char* p){ if(POL==1) return __builtin_nontemporal_load((const u32x4*)p); return *(const u32x4*)p; }
template<int CTRL,int RM,int BM> DEV float dpp(float old,float x){ return __int_as_float(__builtin_amdgcn_update_dpp(__float_as_int(old),__float_as_int(x),CTRL,RM,BM,false)); }
DEV float kdot(u32x4 k,u32x4 q){ return dot2(k.w,q.w,dot2(k.z,q.z,dot2(k.y,q.y,dot2(k.x,q.x,0.f)))); }
DEV float swp16(float a,float b){ auto r=__builtin_amdgcn_permlane16_swap(__float_as_uint(a),__float_as_uint(b),false,false); return __uint_as_float(r[0])+__uint_as_float(r[1]); }
DEV float red8(const float* s,int lane){
  float a0=swp16(s[0],s[1]),a1=swp16(s[2],s[3]),a2=swp16(s[4],s[5]),a3=swp16(s[6],s[7]);
  a0+=dpp<0x128,0xf,0xf>(0.f,a0); a1+=dpp<0x128,0xf,0xf>(0.f,a1); a2+=dpp<0x128,0xf,0xf>(0.f,a2); a3+=dpp<0x128,0xf,0xf>(0.f,a3);
  float b0=(lane&8)?a1:a0, b1=(lane&8)?a3:a2;
  b0+=dpp<0x141,0xf,0xf>(0.f,b0); b1+=dpp<0x141,0xf,0xf>(0.f,b1);
  float c=(lane&4)?b1:b0;
  c+=dpp<0xb1,0xf,0xf>(0.f,c); c+=dpp<0x4e,0xf,0xf>(0.f,c);
  return c;
}
DEV float wmax(float v){
  v=fmaxf(v,dpp<0x141,0xf,0xf>(v,v)); v=fmaxf(v,dpp<0x128,0xf,0xf>(v,v));
  v=fmaxf(v,__shfl_xor(v,16)); v=fmaxf(v,__shfl_xor(v,32)); return v;
}
struct St{ float m,l,acc[8]; };
DEV void soft(float s,St& st,u32* pp){
  if(__builtin_expect(__any(s>st.m),0)){ float mn=fmaxf(st.m,wmax(s)); float f=__builtin_amdgcn_exp2f(st.m-mn); st.m=mn; st.l*=f;
    #pragma unroll
    for(int i=0;i<8;i++) st.acc[i]*=f; }
  float p=__builtin_amdgcn_exp2f(s-st.m); st.l+=p;
  float po=dpp<0x141,0xf,0xf>(0.f,p);
  u32 pr=pk2(p,po);
  pp[0]=__builtin_amdgcn_ds_swizzle(pr,(0)<<5); pp[1]=__builtin_amdgcn_ds_swizzle(pr,(16)<<5);
  pp[2]=__builtin_amdgcn_ds_swizzle(pr,(8)<<5); pp[3]=__builtin_amdgcn_ds_swizzle(pr,(24)<<5);
}
DEV void pv(u32x4 a,u32x4 b,u32 pp,St& st){
  #pragma unroll
  for(int c=0;c<4;c++){ st.acc[2*c]=dot2(__builtin_amdgcn_perm(b[c],a[c],0x05040100),pp,st.acc[2*c]); st.acc[2*c+1]=dot2(__builtin_amdgcn_perm(b[c],a[c],0x07060302),pp,st.acc[2*c+1]); }
}
__global__ __launch_bounds__(256,1) void k_attn(const char* K,const char* V,const u16* q,float* part){
  int lane=threadIdx.x&63,w=threadIdx.x>>6,bh=blockIdx.x>>3,sp=(blockIdx.x&7)*4+w;
  u32x4 qq=*(const u32x4*)(q+bh*256+(lane&31)*8);
  size_t off=((size_t)bh*16384+sp*512)*512+lane*16; const char* kp=K+off; const char* vp=V+off;
  St st; st.m=-1e30f; st.l=0; for(int i=0;i<8;i++) st.acc[i]=0;
  const float sc=0.0625f*1.4426950408889634f;
  u32x4 r[8];
  #pragma unroll
  for(int i=0;i<8;i++) r[i]=ldg(kp+i*1024);
  for(int t=0;t<32;t++){
    float s[8]; u32 pp[4];
    #pragma unroll
    for(int i=0;i<8;i++){ int j=(i>>1)|((i&1)<<2); s[i]=kdot(r[i],qq); r[i]=ldg(vp+t*8192+j*1024); }
    soft(red8(s,lane)*sc,st,pp);
    int tn=t<31?t+1:t;
    #pragma unroll
    for(int j=0;j<4;j++){ pv(r[2*j],r[2*j+1],pp[j],st); r[2*j]=ldg(kp+tn*8192+(2*j)*1024); r[2*j+1]=ldg(kp+tn*8192+(2*j+1)*1024); }
  }
  float l=st.l; for(int o=1;o<64;o<<=1) l+=__shfl_xor(l,o);
  float* pw=part+((size_t)bh*32+sp)*258;
  #pragma unroll
  for(int i=0;i<8;i++){ float a=st.acc[i]+__shfl_xor(st.acc[i],32); if(lane<32) pw[lane*8+i]=a; }
  if(lane==0){ pw[256]=st.m; pw[257]=l*0.25f; }
}
DEV float bsum(float v,float* sm){ for(int o=1;o<64;o<<=1) v+=__shfl_xor(v,o); __syncthreads(); if((threadIdx.x&63)==0) sm[threadIdx.x>>6]=v; __syncthreads(); float t=0; for(int i=0;i<(int)blockDim.x/64;i++) t+=sm[i]; return t; }
DEV float rbf(float x){ return (float)(__bf16)x; }
DEV void ln(const float* x,float* xn,float* sm){ float v=x[threadIdx.x]; float m=bsum(v,sm)*(1.f/448); float d=v-m; float var=bsum(d*d,sm)*(1.f/448); xn[threadIdx.x]=rbf(d*rsqrtf(var+1e-5f)); __syncthreads(); }
__global__ void k_init(const u16* x,float* xf){ xf[threadIdx.x]=bf2f(x[threadIdx.x]); }
__global__ __launch_bounds__(448) void k_q(const float* xf,const u16* wq,u16* q){ __shared__ float xn[448],sm[8],red[7][64]; int b=blockIdx.y; ln(xf+b*448,xn,sm);
  int c=blockIdx.x*64+(threadIdx.x&63),w=threadIdx.x>>6; float a=0; for(int k=w*64;k<w*64+64;k++) a+=xn[k]*bf2f(wq[k*4096+c]); red[w][threadIdx.x&63]=a; __syncthreads();
  if(w==0){ a=0; for(int i=0;i<7;i++) a+=red[i][threadIdx.x]; __bf16 h=(__bf16)a; q[b*4096+c]=__builtin_bit_cast(u16,h);} }
__global__ __launch_bounds__(448) void k_o(const float* part,const u16* wo,float* y){ __shared__ float o[64]; int bh=blockIdx.x>>2,qd=blockIdx.x&3; const float* p=part+(size_t)bh*32*258;
  if(threadIdx.x<64){ float m=-1e30f; for(int s=0;s<32;s++) m=fmaxf(m,p[s*258+256]); float l=0,a=0; for(int s=0;s<32;s++){ float f=exp2f(p[s*258+256]-m); l+=f*p[s*258+257]; a+=f*p[s*258+qd*64+threadIdx.x]; } o[threadIdx.x]=rbf(a/l);} __syncthreads();
  float a=0; const u16* w=wo+(size_t)((bh&15)*256+qd*64)*448+threadIdx.x; for(int k=0;k<64;k++) a+=o[k]*bf2f(w[k*448]); y[blockIdx.x*448+threadIdx.x]=a; }
__global__ __launch_bounds__(448) void k_add(float* xf,const float* y,int n,u16* out){ int b=blockIdx.x; float a=0; for(int i=0;i<n;i++) a+=y[(b*n+i)*448+threadIdx.x]; float v=rbf(xf[b*448+threadIdx.x]+rbf(a)); xf[b*448+threadIdx.x]=v; if(out){ __bf16 h=(__bf16)v; out[b*448+threadIdx.x]=__builtin_bit_cast(u16,h);} }
__global__ __launch_bounds__(448) void k_mlp(const float* xf,const u16* wg,const u16* wu,const u16* wd,float* y){ __shared__ float xn[448],sm[8],rg[7][64],ru[7][64],h[64]; int b=blockIdx.y; ln(xf+b*448,xn,sm);
  int c=blockIdx.x*64+(threadIdx.x&63),w=threadIdx.x>>6; float g=0,u=0; for(int k=w*64;k<w*64+64;k++){ g+=xn[k]*bf2f(wg[k*1792+c]); u+=xn[k]*bf2f(wu[k*1792+c]); } rg[w][threadIdx.x&63]=g; ru[w][threadIdx.x&63]=u; __syncthreads();
  if(w==0){ g=0;u=0; for(int i=0;i<7;i++){ g+=rg[i][threadIdx.x]; u+=ru[i][threadIdx.x]; } g=rbf(g); u=rbf(u); h[threadIdx.x]=rbf(rbf(1.f/(1.f+__expf(-g)))*u);} __syncthreads();
  float a=0; const u16* ww=wd+(size_t)blockIdx.x*64*448+threadIdx.x; for(int k=0;k<64;k++) a+=h[k]*bf2f(ww[k*448]); y[(b*28+blockIdx.x)*448+threadIdx.x]=a; }
extern "C" void kernel_launch(void* const* d_in,const int* in_sizes,int n_in,void* d_out,int out_size,void* d_ws,size_t ws_size,hipStream_t stream){
  const u16* x=(const u16*)d_in[0]; const char* K=(const char*)d_in[1]; const char* V=(const char*)d_in[2]; const u16* wq=(const u16*)d_in[3]; const u16* wo=(const u16*)d_in[4];
  const u16* wg=(const u16*)d_in[6]; const u16* wu=(const u16*)d_in[7]; const u16* wd=(const u16*)d_in[8];
  char* ws=(char*)d_ws; float* xf=(float*)ws; u16* q=(u16*)(ws+4096); float* y=(float*)(ws+65536); float* part=(float*)(ws+(1<<20));
  k_init<<<1,896,0,stream>>>(x,xf);
  for(int L=0;L<2;L++){
    k_q<<<dim3(64,2),448,0,stream>>>(xf,wq,q);
    k_attn<<<256,256,0,stream>>>(K,V,q,part);
    k_o<<<128,448,0,stream>>>(part,wo,y);
    k_add<<<2,448,0,stream>>>(xf,y,64,nullptr);
    k_mlp<<<dim3(28,2),448,0,stream>>>(xf,wg,wu,wd,y);
    k_add<<<2,448,0,stream>>>(xf,y,28,L==1?(u16*)d_out:nullptr);
  }
}
```

```cpp
#include <hip/hip_runtime.h>
#include <hip/hip_bf16.h>
typedef unsigned u32;
typedef unsigned short u16;
typedef u32 u32x4 __attribute__((ext_vector_type(4)));
typedef u32 u32x2 __attribute__((ext_vector_type(2)));
typedef float f32x4 __attribute__((ext_vector_type(4)));
typedef __bf16 bf16x2 __attribute__((ext_vector_type(2)));
typedef __bf16 bf16x8 __attribute__((ext_vector_type(8)));
typedef __amdgpu_buffer_rsrc_t rsrc_t;
#define DEV __device__ __forceinline__
#define LDSP __attribute__((address_space(3)))
#define PR "nt"
#define PS1 "nt"
constexpr u32 WS_Q = 0;
constexpr u32 WS_Y = 65536;
constexpr u32 WS_ZEND = WS_Y + 4*2*16*1792;
constexpr u32 WS_ML = 0x80000;
constexpr u32 WS_P = 0x100000;
constexpr u32 WS_END = WS_P + 4*16*64*1024;
constexpr int RING = 64, VR0 = 96;
constexpr u32 SPIN_MAX = 1u << 20;

DEV float bf2f(u16 v){ return __uint_as_float((u32)v<<16); }
DEV float rbf(float x){ return (float)(__bf16)x; }
DEV float dot2(u32 a,u32 b,float c){ return __builtin_amdgcn_fdot2_f32_bf16(__builtin_bit_cast(bf16x2,a),__builtin_bit_cast(bf16x2,b),c,false); }
DEV u32 pk2(float a,float b){ bf16x2 v; v.x=(__bf16)a; v.y=(__bf16)b; return __builtin_bit_cast(u32,v); }
DEV u32 plo(u32 a,u32 b){ return __builtin_amdgcn_perm(b,a,0x05040100); }
DEV u32 phi(u32 a,u32 b){ return __builtin_amdgcn_perm(b,a,0x07060302); }
template<int CTRL> DEV float dpp(float x){ return __int_as_float(__builtin_amdgcn_update_dpp(0,__float_as_int(x),CTRL,0xf,0xf,true)); }
DEV float kdot(u32 k0,u32 k1,u32 k2,u32 k3,u32x4 q){ return dot2(k3,q.w,dot2(k2,q.z,dot2(k1,q.y,dot2(k0,q.x,0.f)))); }
DEV float swp16(float a,float b){ auto r=__builtin_amdgcn_permlane16_swap(__float_as_uint(a),__float_as_uint(b),false,false); return __uint_as_float(r[0])+__uint_as_float(r[1]); }
DEV float swp32(float a,float b){ auto r=__builtin_amdgcn_permlane32_swap(__float_as_uint(a),__float_as_uint(b),false,false); return __uint_as_float(r[0])+__uint_as_float(r[1]); }
DEV float wsum(float v){ v+=dpp<0xb1>(v); v+=dpp<0x4e>(v); v+=dpp<0x141>(v); v+=dpp<0x128>(v); v=swp16(v,v); return swp32(v,v); }
DEV float wmax(float v){
  v=fmaxf(v,dpp<0xb1>(v)); v=fmaxf(v,dpp<0x4e>(v)); v=fmaxf(v,dpp<0x141>(v)); v=fmaxf(v,dpp<0x128>(v));
  auto r=__builtin_amdgcn_permlane16_swap(__float_as_uint(v),__float_as_uint(v),false,false); v=fmaxf(__uint_as_float(r[0]),__uint_as_float(r[1]));
  r=__builtin_amdgcn_permlane32_swap(__float_as_uint(v),__float_as_uint(v),false,false); return fmaxf(__uint_as_float(r[0]),__uint_as_float(r[1]));
}
struct St{ float m,l,acc[8]; };
DEV void st_reset(St& s){ s.m=-1e30f; s.l=0.f;
  #pragma unroll
  for(int i=0;i<8;i++) s.acc[i]=0.f; }
DEV int lane_id(){ int l; asm volatile("v_mbcnt_lo_u32_b32 %0, -1, 0\n\tv_mbcnt_hi_u32_b32 %0, -1, %0" : "=v"(l)); return l; }
DEV float uni(float x){ return __int_as_float(__builtin_amdgcn_readfirstlane(__float_as_int(x))); }
DEV u32 soft(float a0,float a1,float a2,float a3,int lane,St& st){
  a0+=dpp<0x128>(a0); a1+=dpp<0x128>(a1); a2+=dpp<0x128>(a2); a3+=dpp<0x128>(a3);
  float b0=(lane&8)?a1:a0, b1=(lane&8)?a3:a2;
  b0+=dpp<0x141>(b0); b1+=dpp<0x141>(b1);
  float c=(lane&4)?b1:b0;
  c+=dpp<0xb1>(c); c+=dpp<0x4e>(c);
  float s=rbf(c)*(0.0625f*1.4426950408889634f);
  if(__builtin_expect(__any(s>st.m),0)){
    float mn=uni(fmaxf(st.m,wmax(s))); float f=__builtin_amdgcn_exp2f(st.m-mn); st.m=mn; st.l*=f;
    #pragma unroll
    for(int i=0;i<8;i++) st.acc[i]*=f; }
  float p=__builtin_amdgcn_exp2f(s-st.m); st.l+=p;
  return pk2(p,dpp<0x141>(p));
}
DEV void spread(u32 pr,u32* pp){
  pp[0]=__builtin_amdgcn_ds_swizzle(pr,0<<5); pp[1]=__builtin_amdgcn_ds_swizzle(pr,16<<5);
  pp[2]=__builtin_amdgcn_ds_swizzle(pr,8<<5); pp[3]=__builtin_amdgcn_ds_swizzle(pr,24<<5);
}
DEV void pin(St& s){ asm volatile("" : "+v"(s.acc[0]),"+v"(s.acc[1]),"+v"(s.acc[2]),"+v"(s.acc[3]),"+v"(s.acc[4]),"+v"(s.acc[5]),"+v"(s.acc[6]),"+v"(s.acc[7])); }
DEV float pinf(float x){ asm volatile("" : "+v"(x)); return x; }
DEV void pvr(const u32* a,const u32* b,u32 pp,St& st){
  #pragma unroll
  for(int c=0;c<4;c++){ st.acc[2*c]=dot2(plo(a[c],b[c]),pp,st.acc[2*c]); st.acc[2*c+1]=dot2(phi(a[c],b[c]),pp,st.acc[2*c+1]); }
  pin(st);
}
#define WAITV(n) do{ __builtin_amdgcn_sched_barrier(0); asm volatile("s_waitcnt vmcnt(" #n ")" ::: "memory"); }while(0)
template<int R> DEV void rdA8(u32* t,int){ asm volatile(
  "v_accvgpr_read_b32 %0, a[%8]\n\tv_accvgpr_read_b32 %1, a[%8+1]\n\tv_accvgpr_read_b32 %2, a[%8+2]\n\tv_accvgpr_read_b32 %3, a[%8+3]\n\t"
  "v_accvgpr_read_b32 %4, a[%8+4]\n\tv_accvgpr_read_b32 %5, a[%8+5]\n\tv_accvgpr_read_b32 %6, a[%8+6]\n\tv_accvgpr_read_b32 %7, a[%8+7]"
  : "=v"(t[0]),"=v"(t[1]),"=v"(t[2]),"=v"(t[3]),"=v"(t[4]),"=v"(t[5]),"=v"(t[6]),"=v"(t[7]) : "n"(R)); }
template<int R> DEV float dotV2(u32x4 q){ float s0,s1; asm volatile(
  "v_dot2_f32_bf16 %0, v[%6], %2, 0\n\tv_dot2_f32_bf16 %1, v[%6+4], %2, 0\n\tv_dot2_f32_bf16 %0, v[%6+1], %3, %0\n\tv_dot2_f32_bf16 %1, v[%6+5], %3, %1\n\t"
  "v_dot2_f32_bf16 %0, v[%6+2], %4, %0\n\tv_dot2_f32_bf16 %1, v[%6+6], %4, %1\n\tv_dot2_f32_bf16 %0, v[%6+3], %5, %0\n\tv_dot2_f32_bf16 %1, v[%6+7], %5, %1\n\ts_nop 2"
  : "=&v"(s0),"=&v"(s1) : "v"(q.x),"v"(q.y),"v"(q.z),"v"(q.w),"n"(R)); return swp16(s0,s1); }
template<int R> DEV void permV2(u32* t){ asm volatile(
  "v_perm_b32 %0, v[%10+4], v[%10], %8\n\tv_perm_b32 %1, v[%10+4], v[%10], %9\n\tv_perm_b32 %2, v[%10+5], v[%10+1], %8\n\tv_perm_b32 %3, v[%10+5], v[%10+1], %9\n\t"
  "v_perm_b32 %4, v[%10+6], v[%10+2], %8\n\tv_perm_b32 %5, v[%10+6], v[%10+2], %9\n\tv_perm_b32 %6, v[%10+7], v[%10+3], %8\n\tv_perm_b32 %7, v[%10+7], v[%10+3], %9"
  : "=v"(t[0]),"=v"(t[1]),"=v"(t[2]),"=v"(t[3]),"=v"(t[4]),"=v"(t[5]),"=v"(t[6]),"=v"(t[7]) : "s"(0x05040100u),"s"(0x07060302u),"n"(R)); }
DEV void pvt(const u32* t,u32 pp,St& st){
  #pragma unroll
  for(int c=0;c<8;c++) st.acc[c]=dot2(t[c],pp,st.acc[c]);
  pin(st); }
#define LD2(F,POL) asm volatile("global_load_dwordx4 " F "[%2:%2+3], %0, %1 offset:%3 " POL "\n\tglobal_load_dwordx4 " F "[%2+4:%2+7], %0, %1 offset:%4 " POL \
  :: "v"(vo),"s"(mid),"n"(R),"n"(O0),"n"(O1) : "memory")
template<int R,int O0,int O1> DEV void ldV2r(u32 vo,const char* mid){ LD2("v",PR); }
template<int R,int O0,int O1> DEV void ldA2r(u32 vo,const char* mid){ LD2("a",PR); }
template<int R,int O0,int O1> DEV void ldV2s(u32 vo,const char* mid){ LD2("v",PS1); }
template<bool ISV,int I> struct Off{ static constexpr int a=(ISV?I:2*I)*1024-4096, b=(ISV?I+4:2*I+1)*1024-4096; };
template<int G> DEV void issue(u32 vo,const char* mid,u32 ldsw){
  constexpr bool V=G&1;
  if constexpr(G<8){ constexpr int R=32*G; ldA2r<R,Off<V,0>::a,Off<V,0>::b>(vo,mid); ldA2r<R+8,Off<V,1>::a,Off<V,1>::b>(vo,mid); ldA2r<R+16,Off<V,2>::a,Off<V,2>::b>(vo,mid); ldA2r<R+24,Off<V,3>::a,Off<V,3>::b>(vo,mid); }
  else if constexpr(G<13){ constexpr int R=VR0+32*(G-8); ldV2r<R,Off<V,0>::a,Off<V,0>::b>(vo,mid); ldV2r<R+8,Off<V,1>::a,Off<V,1>::b>(vo,mid); ldV2r<R+16,Off<V,2>::a,Off<V,2>::b>(vo,mid); ldV2r<R+24,Off<V,3>::a,Off<V,3>::b>(vo,mid); }
  else { u32 m0a=ldsw+(G-13)*8192; const char* lo=mid-4096;
    asm volatile("s_waitcnt lgkmcnt(0)\n\ts_mov_b32 m0, %3\n\ts_nop 0\n\t"
      "global_load_lds_dwordx4 %0, %1 " PR "\n\tglobal_load_lds_dwordx4 %0, %1 offset:1024 " PR "\n\tglobal_load_lds_dwordx4 %0, %1 offset:2048 " PR "\n\tglobal_load_lds_dwordx4 %0, %1 offset:3072 " PR "\n\t"
      "s_mov_b32 m0, %4\n\ts_nop 0\n\t"
      "global_load_lds_dwordx4 %0, %2 " PR "\n\tglobal_load_lds_dwordx4 %0, %2 offset:1024 " PR "\n\tglobal_load_lds_dwordx4 %0, %2 offset:2048 " PR "\n\tglobal_load_lds_dwordx4 %0, %2 offset:3072 " PR
      :: "v"(vo),"s"(lo),"s"(mid),"s"(m0a),"s"(m0a+4096) : "memory","m0"); }
}
#define SB __builtin_amdgcn_sched_barrier(0)
template<int G,int I> DEV float kpair(u32x4 q,const LDSP u32x4* lw){ SB;
  if constexpr(G<8){ u32 t[8]; rdA8<32*G+8*I>(t,0); return pinf(swp16(kdot(t[0],t[1],t[2],t[3],q),kdot(t[4],t[5],t[6],t[7],q))); }
  else if constexpr(G<13) return dotV2<VR0+32*(G-8)+8*I>(q);
  else { u32x4 a=lw[(G-13)*512+(2*I)*64], b=lw[(G-13)*512+(2*I+1)*64]; return pinf(swp16(kdot(a.x,a.y,a.z,a.w,q),kdot(b.x,b.y,b.z,b.w,q))); }
}
template<int G,int I> DEV void vpair(u32 pp,St& st,const LDSP u32x4* lw){
  u32 t[8]; SB;
  if constexpr(G<8){ rdA8<32*G+8*I>(t,0); pvr(t,t+4,pp,st); }
  else if constexpr(G<13){ permV2<VR0+32*(G-8)+8*I>(t); pvt(t,pp,st); }
  else { u32x4 a=lw[(G-13)*512+I*64], b=lw[(G-13)*512+(I+4)*64]; t[0]=a.x;t[1]=a.y;t[2]=a.z;t[3]=a.w;t[4]=b.x;t[5]=b.y;t[6]=b.z;t[7]=b.w; pvr(t,t+4,pp,st); }
}
template<int G> DEV void comp(u32x4 q,St& st,u32& pr,int lane,const LDSP u32x4* lw){
  if constexpr(!(G&1)){ float a0=kpair<G,0>(q,lw),a1=kpair<G,1>(q,lw),a2=kpair<G,2>(q,lw),a3=kpair<G,3>(q,lw); pr=soft(a0,a1,a2,a3,lane,st); asm volatile("" : "+v"(pr)); }
  else { u32 pp[4]; asm volatile("" : "+v"(pr)); spread(pr,pp); vpair<G,0>(pp[0],st,lw); vpair<G,1>(pp[1],st,lw); vpair<G,2>(pp[2],st,lw); vpair<G,3>(pp[3],st,lw); }
}
DEV void ringK(u32 vo,const char* mid){ ldV2s<RING,-4096,-3072>(vo,mid); ldV2s<RING+8,-2048,-1024>(vo,mid); ldV2s<RING+16,0,1024>(vo,mid); ldV2s<RING+24,2048,3072>(vo,mid); }
struct Cx{ u32x4 qO,qN; St sO,sN; u32 ppO,ppN; const char *kn,*vn,*ringnext; int it,lane; u32 vo,ldsw; const LDSP u32x4* lw; };
template<int G> DEV void rstep(Cx& c){
  if constexpr(G<18){
    if(c.it!=0) comp<G>(c.qO,c.sO,c.ppO,c.lane,c.lw);
    const char* mid=((G&1)?c.vn:c.kn)+(G>>1)*8192+4096;
    if constexpr(G<6){ if(c.it!=0){ issue<G>(c.vo,mid,c.ldsw); WAITV(8); } else if constexpr(G==5) WAITV(8); }
    else if constexpr(G<16){ issue<G>(c.vo,mid,c.ldsw); WAITV(8); }
    else { if(c.it!=2){ issue<G>(c.vo,mid,c.ldsw); WAITV(8); } else WAITV(0); }
  } else { if(c.it!=2){ ringK(c.vo,c.ringnext); WAITV(8); } }
  if constexpr(G>=1 && G<=16) comp<G-1>(c.qN,c.sN,c.ppN,c.lane,c.lw);
  if constexpr(G>16){ if(c.it!=2) comp<G-1>(c.qN,c.sN,c.ppN,c.lane,c.lw); }
  if constexpr(G<18) rstep<G+1>(c);
}
DEV void stream(Cx& c,const char* kmid,const char* vmid,const char* nextk,bool has_next){
  #pragma nounroll
  for(int t=0;t<8;t++){
    WAITV(6); float a0=dotV2<RING>(c.qN);    ldV2s<RING,   Off<1,0>::a,Off<1,0>::b>(c.vo,vmid);
    WAITV(6); float a1=dotV2<RING+8>(c.qN);  ldV2s<RING+8, Off<1,1>::a,Off<1,1>::b>(c.vo,vmid);
    WAITV(6); float a2=dotV2<RING+16>(c.qN); ldV2s<RING+16,Off<1,2>::a,Off<1,2>::b>(c.vo,vmid);
    WAITV(6); float a3=dotV2<RING+24>(c.qN); ldV2s<RING+24,Off<1,3>::a,Off<1,3>::b>(c.vo,vmid);
    u32 pp[4]; spread(soft(a0,a1,a2,a3,c.lane,c.sN),pp);
    kmid+=8192; vmid+=8192; if(t==7) kmid=nextk;
    bool pf=(t<7)||has_next; u32 x[8];
    if(!pf) WAITV(0);
    WAITV(6); permV2<RING>(x);    if(pf) ldV2s<RING,-4096,-3072>(c.vo,kmid);   pvt(x,pp[0],c.sN);
    WAITV(6); permV2<RING+8>(x);  if(pf) ldV2s<RING+8,-2048,-1024>(c.vo,kmid); pvt(x,pp[1],c.sN);
    WAITV(6); permV2<RING+16>(x); if(pf) ldV2s<RING+16,0,1024>(c.vo,kmid);     pvt(x,pp[2],c.sN);
    WAITV(6); permV2<RING+24>(x); if(pf) ldV2s<RING+24,2048,3072>(c.vo,kmid);  pvt(x,pp[3],c.sN);
  }
}
DEV u32x4 ld16(rsrc_t r,u32 off,u32 so=0){ return __builtin_amdgcn_raw_buffer_load_b128(r,off,so,16); }
DEV void st16(rsrc_t r,u32 off,u32x4 v,u32 so=0){ __builtin_amdgcn_raw_buffer_store_b128(v,r,off,so,16); }
DEV u32x4 tag4(float a,float b,float c,float d){ u32x4 v={__float_as_uint(a)|1u,__float_as_uint(b)|1u,__float_as_uint(c)|1u,__float_as_uint(d)|1u}; return v; }
DEV u32 and4(u32x4 v){ return v.x&v.y&v.z&v.w; }
DEV bool retry(u32 ok,u32& spins){ if(__all(ok&1u)) return false; if(++spins>SPIN_MAX) return false; __builtin_amdgcn_s_sleep(2); asm volatile("":::"memory"); return true; }
DEV u32x4 pollq(rsrc_t ws,int step,int h,u32& spins){
  int lane=lane_id(); u32 off=(lane&31)*32, so=WS_Q+(step*16+h)*1024; u32x4 a,b;
  do{ a=ld16(ws,off,so); b=ld16(ws,off+16,so); } while(retry(and4(a)&and4(b),spins));
  u32x4 q={phi(a.x,a.y),phi(a.z,a.w),phi(b.x,b.y),phi(b.z,b.w)}; return q;
}
DEV void publish(rsrc_t ws,const St& s,int step,int h,int slot){
  u32 idx=(step*16+h)*64+slot;
  int lane=lane_id();
  st16(ws,lane*16,tag4(swp32(s.acc[0],s.acc[4]),swp32(s.acc[1],s.acc[5]),swp32(s.acc[2],s.acc[6]),swp32(s.acc[3],s.acc[7])),WS_P+idx*1024);
  float l=wsum(s.l)*0.25f;
  if(lane==0){ u32x2 v={__float_as_uint(s.m)|1u,__float_as_uint(l)|1u}; __builtin_amdgcn_raw_buffer_store_b64(v,ws,0,WS_ML+idx*8,16); }
}
struct Args{ const u16 *x,*wq,*wo,*wg,*wu,*wd; const char *K,*V; char* ws; u16* out; };
DEV void streamer(const Args& A,rsrc_t ws,LDSP char* smem,int lane,int w){
  const int idx=blockIdx.x-16, h=idx&15, slot=(idx>>4)*4+w;
  const size_t BS=(size_t)16*16384*512, off=((size_t)h*16384+slot*272)*512;
  const char *k0=A.K+off,*v0=A.V+off; constexpr int S=9*8192+4096;
  Cx c; c.lane=lane; c.vo=lane*16; c.ldsw=(u32)(size_t)smem+w*40960; c.lw=(const LDSP u32x4*)(smem+w*40960)+lane;
  u32 spins=0;
  issue<0>(c.vo,k0+4096,0); issue<1>(c.vo,v0+4096,0); WAITV(8); issue<2>(c.vo,k0+8192+4096,0); WAITV(8); issue<3>(c.vo,v0+8192+4096,0); WAITV(8);
  c.qN=pollq(ws,0,h,spins); c.qO=pollq(ws,1,h,spins);
  issue<4>(c.vo,k0+16384+4096,0); issue<5>(c.vo,v0+16384+4096,0);
  st_reset(c.sN); st_reset(c.sO); c.kn=k0; c.vn=v0; c.ringnext=k0+S;
  c.ppO=0; c.ppN=0;
  #pragma nounroll
  for(int it=0;it<3;it++){
    c.it=it; rstep<0>(c);
    if(it==0){
      stream(c,k0+S,v0+S,k0+BS+S,true); publish(ws,c.sN,0,h,slot);
      st_reset(c.sN); c.qN=c.qO; stream(c,k0+BS+S,v0+BS+S,k0,false);
      c.qO=pollq(ws,2,h,spins); c.kn=k0+BS; c.vn=v0+BS;
    } else if(it==1){
      publish(ws,c.sN,1,h,slot); c.sN=c.sO; c.qN=c.qO;
      stream(c,k0+S,v0+S,k0,false); publish(ws,c.sN,2,h,slot);
      c.qO=pollq(ws,3,h,spins); c.qN=c.qO; st_reset(c.sN); st_reset(c.sO); c.kn=k0+BS+9*8192; c.vn=v0+BS+9*8192;
    } else {
      float m=fmaxf(c.sN.m,c.sO.m), fn=__builtin_amdgcn_exp2f(c.sN.m-m), fo=__builtin_amdgcn_exp2f(c.sO.m-m);
      c.sN.m=m; c.sN.l=c.sN.l*fn+c.sO.l*fo;
      #pragma unroll
      for(int i=0;i<8;i++) c.sN.acc[i]=c.sN.acc[i]*fn+c.sO.acc[i]*fo;
      publish(ws,c.sN,3,h,slot);
    }
  }
}
constexpr int L_WD=0, L_GU=100352, L_RED=129024  , L_XN=136192  , L_RED2=143360  , L_OV=147456  , L_HV=147968  , L_XR=148992  , L_RED3=152576  ;
template<bool AG,int R> DEV void ldw1(u32 vo,const char* p){
  if constexpr(AG) asm volatile("global_load_dwordx4 a[%2:%2+3], %0, %1" :: "v"(vo),"s"(p),"n"(R) : "memory");
  else asm volatile("global_load_dwordx4 v[%2:%2+3], %0, %1" :: "v"(vo),"s"(p),"n"(R) : "memory"); }
template<bool AG,int R,int N,int I=0> DEV void ldwn(u32 vo,const char* p,int rowb){ if constexpr(I<N){ asm volatile("" : "+s"(p)); ldw1<AG,R+4*I>(vo,p); ldwn<AG,R,N,I+1>(vo,p+((I&7)==7?25:1)*rowb,rowb); } }
template<int R> DEV void rdV8(u32* t){ asm volatile(
  "v_mov_b32 %0, v[%8]\n\tv_mov_b32 %1, v[%8+1]\n\tv_mov_b32 %2, v[%8+2]\n\tv_mov_b32 %3, v[%8+3]\n\tv_mov_b32 %4, v[%8+4]\n\tv_mov_b32 %5, v[%8+5]\n\tv_mov_b32 %6, v[%8+6]\n\tv_mov_b32 %7, v[%8+7]"
  : "=v"(t[0]),"=v"(t[1]),"=v"(t[2]),"=v"(t[3]),"=v"(t[4]),"=v"(t[5]),"=v"(t[6]),"=v"(t[7]) : "n"(R)); }
template<bool AG,int R> DEV void wr4(u32x4 v){
  if constexpr(AG) asm volatile("v_accvgpr_write_b32 a[%4], %0\n\tv_accvgpr_write_b32 a[%4+1], %1\n\tv_accvgpr_write_b32 a[%4+2], %2\n\tv_accvgpr_write_b32 a[%4+3], %3" :: "v"(v.x),"v"(v.y),"v"(v.z),"v"(v.w),"n"(R));
  else asm volatile("v_mov_b32 v[%4], %0\n\tv_mov_b32 v[%4+1], %1\n\tv_mov_b32 v[%4+2], %2\n\tv_mov_b32 v[%4+3], %3" :: "v"(v.x),"v"(v.y),"v"(v.z),"v"(v.w),"n"(R)); }
template<int E> DEV u32x4 colop(const u32* t){ constexpr int c=E>>1; u32x4 v;
  if constexpr(E&1){ v.x=phi(t[c],t[4+c]); v.y=phi(t[8+c],t[12+c]); v.z=phi(t[16+c],t[20+c]); v.w=phi(t[24+c],t[28+c]); }
  else { v.x=plo(t[c],t[4+c]); v.y=plo(t[8+c],t[12+c]); v.z=plo(t[16+c],t[20+c]); v.w=plo(t[24+c],t[28+c]); } return v; }
template<bool AG,int R,int NKB> DEV void permute(){ if constexpr(NKB>0){ u32 t[32];
  if constexpr(AG){ rdA8<R>(t,0); rdA8<R+8>(t+8,0); rdA8<R+16>(t+16,0); rdA8<R+24>(t+24,0); } else { rdV8<R>(t); rdV8<R+8>(t+8); rdV8<R+16>(t+16); rdV8<R+24>(t+24); }
  wr4<AG,R>(colop<0>(t)); wr4<AG,R+4>(colop<1>(t)); wr4<AG,R+8>(colop<2>(t)); wr4<AG,R+12>(colop<3>(t)); wr4<AG,R+16>(colop<4>(t)); wr4<AG,R+20>(colop<5>(t)); wr4<AG,R+24>(colop<6>(t)); wr4<AG,R+28>(colop<7>(t));
  permute<AG,R+32,NKB-1>(); } }
#define MF8(F) asm volatile("s_nop 1\n\tv_mfma_f32_16x16x32_bf16 %0, %8, " F "[%9:%9+3], %0\n\tv_mfma_f32_16x16x32_bf16 %1, %8, " F "[%9+4:%9+7], %1\n\t" \
  "v_mfma_f32_16x16x32_bf16 %2, %8, " F "[%9+8:%9+11], %2\n\tv_mfma_f32_16x16x32_bf16 %3, %8, " F "[%9+12:%9+15], %3\n\tv_mfma_f32_16x16x32_bf16 %4, %8, " F "[%9+16:%9+19], %4\n\t" \
  "v_mfma_f32_16x16x32_bf16 %5, %8, " F "[%9+20:%9+23], %5\n\tv_mfma_f32_16x16x32_bf16 %6, %8, " F "[%9+24:%9+27], %6\n\tv_mfma_f32_16x16x32_bf16 %7, %8, " F "[%9+28:%9+31], %7\n\ts_nop 7" \
  : "+v"(d[0]),"+v"(d[1]),"+v"(d[2]),"+v"(d[3]),"+v"(d[4]),"+v"(d[5]),"+v"(d[6]),"+v"(d[7]) : "v"(a),"n"(R))
template<bool AG,int R,int NKB> DEV void gemv(f32x4* d,const LDSP u32x4* xa){ if constexpr(NKB>0){ u32x4 a=xa[0]; if constexpr(AG) MF8("a"); else MF8("v"); gemv<AG,R+32,NKB-1>(d,xa+4); } }
DEV void mf2(f32x4& d0,f32x4& d1,u32x4 a,u32x4 b0,u32x4 b1){ asm volatile("s_nop 1\n\tv_mfma_f32_16x16x32_bf16 %0, %2, %3, %0\n\tv_mfma_f32_16x16x32_bf16 %1, %2, %4, %1\n\ts_nop 7" : "+v"(d0),"+v"(d1) : "v"(a),"v"(b0),"v"(b1)); }
DEV void zero8(f32x4* d){
  #pragma unroll
  for(int e=0;e<8;e++) d[e]=f32x4{0.f,0.f,0.f,0.f}; }
DEV f32x4 pick4(const f32x4* d,bool hi){ return f32x4{hi?d[4].x:d[0].x,hi?d[5].x:d[1].x,hi?d[6].x:d[2].x,hi?d[7].x:d[3].x}; }
DEV void kb_to_lds(const char* p,u32 vo,int rowb,LDSP char* dst,int estride,bool wr){
  u32 t[32];
  #pragma unroll
  for(int j=0;j<8;j++){ asm volatile("" : "+s"(p)); u32x4 v=*(const u32x4*)(p+vo); p+=rowb; t[4*j]=v.x; t[4*j+1]=v.y; t[4*j+2]=v.z; t[4*j+3]=v.w; }
  if(wr){ *(LDSP u32x4*)(dst)=colop<0>(t); *(LDSP u32x4*)(dst+estride)=colop<1>(t); asm volatile("":::"memory"); SB; *(LDSP u32x4*)(dst+2*estride)=colop<2>(t); *(LDSP u32x4*)(dst+3*estride)=colop<3>(t);
          asm volatile("":::"memory"); SB; *(LDSP u32x4*)(dst+4*estride)=colop<4>(t); *(LDSP u32x4*)(dst+5*estride)=colop<5>(t); asm volatile("":::"memory"); SB; *(LDSP u32x4*)(dst+6*estride)=colop<6>(t); *(LDSP u32x4*)(dst+7*estride)=colop<7>(t); }
}
DEV void lnorm(const float* x,int lane,LDSP char* dst){
  bool on=lane<56; float s=0.f;
  #pragma unroll
  for(int i=0;i<8;i++) s+=x[i];
  float mean=wsum(on?s:0.f)*(1.f/448.f); s=0.f;
  #pragma unroll
  for(int i=0;i<8;i++){ float d=x[i]-mean; s+=d*d; }
  float var=wsum(on?s:0.f)*(1.f/448.f);
  float mb=rbf(mean), den=rbf(sqrtf(rbf(rbf(var)+1e-5f))), r[8];
  #pragma unroll
  for(int i=0;i<8;i++) r[i]=rbf(x[i]-mb)/den;
  if(on) *(LDSP u32x4*)(dst+lane*16)=u32x4{pk2(r[0],r[1]),pk2(r[2],r[3]),pk2(r[4],r[5]),pk2(r[6],r[7])};
}
DEV void gather_y(rsrc_t ws,u32 ybase,LDSP char* smem,int b_,int w,float* x,u32& spins){
  int lane=lane_id(), lc=lane<56?lane:55; u32 off=lc*32, so=ybase+w*4*1792; u32x4 v[8]; u32 ok;
  do{ ok=~0u;
    #pragma unroll
    for(int i=0;i<8;i++){ v[i]=ld16(ws,off+(i&1)*16,so+(i>>1)*1792); ok&=and4(v[i]); }
  } while(retry(ok,spins));
  f32x4 a=__builtin_bit_cast(f32x4,v[0])+__builtin_bit_cast(f32x4,v[2])+__builtin_bit_cast(f32x4,v[4])+__builtin_bit_cast(f32x4,v[6]);
  f32x4 b=__builtin_bit_cast(f32x4,v[1])+__builtin_bit_cast(f32x4,v[3])+__builtin_bit_cast(f32x4,v[5])+__builtin_bit_cast(f32x4,v[7]);
  LDSP f32x4* r=(LDSP f32x4*)(smem+L_RED)+lc*2;
  if(lane<56){ r[w*112]=a; r[w*112+1]=b; }
  LDSP f32x4* xr=(LDSP f32x4*)(smem+L_XR+b_*1792)+lc*2; f32x4 p=xr[0],q=xr[1];
  __syncthreads();
  a=(r[0]+r[112])+(r[224]+r[336]); b=(r[1]+r[113])+(r[225]+r[337]);
  #pragma unroll
  for(int i=0;i<4;i++){ x[i]=rbf(p[i]+rbf(a[i])); x[4+i]=rbf(q[i]+rbf(b[i])); }
  if(w==0&&lane<56){ xr[0]=f32x4{x[0],x[1],x[2],x[3]}; xr[1]=f32x4{x[4],x[5],x[6],x[7]}; }
}
template<int R,int C> DEV void wq2(f32x4* d,u32x4 a){ u32 t[8]; asm volatile(
  "v_accvgpr_read_b32 %0, a[%8]\n\tv_accvgpr_read_b32 %1, a[%8+4]\n\tv_accvgpr_read_b32 %2, a[%8+8]\n\tv_accvgpr_read_b32 %3, a[%8+12]\n\t"
  "v_accvgpr_read_b32 %4, a[%8+16]\n\tv_accvgpr_read_b32 %5, a[%8+20]\n\tv_accvgpr_read_b32 %6, a[%8+24]\n\tv_accvgpr_read_b32 %7, a[%8+28]"
  : "=v"(t[0]),"=v"(t[1]),"=v"(t[2]),"=v"(t[3]),"=v"(t[4]),"=v"(t[5]),"=v"(t[6]),"=v"(t[7]) : "n"(R+C));
  mf2(d[2*C],d[2*C+1],a,u32x4{plo(t[0],t[1]),plo(t[2],t[3]),plo(t[4],t[5]),plo(t[6],t[7])},u32x4{phi(t[0],t[1]),phi(t[2],t[3]),phi(t[4],t[5]),phi(t[6],t[7])}); }
template<int R,int NKB> DEV void gemv_raw(f32x4* d,const LDSP u32x4* xa){ if constexpr(NKB>0){ u32x4 a=xa[0]; wq2<R,0>(d,a); wq2<R,1>(d,a); wq2<R,2>(d,a); wq2<R,3>(d,a); gemv_raw<R+32,NKB-1>(d,xa+4); } }
DEV void zero_p(rsrc_t ws,int step,int h,int tid){
  u32 z0; asm volatile("v_mov_b32 %0, 0" : "=v"(z0)); u32x4 z={z0,z0,z0,z0};
  #pragma unroll
  for(int i=0;i<16;i++) st16(ws,tid*16,z,WS_P+(step*16+h)*65536+i*4096);
  if(tid<32) st16(ws,tid*16,z,WS_ML+(step*16+h)*512);
}
DEV void chain(const Args& A,rsrc_t ws,LDSP char* smem,int lane,int w){
  int h=blockIdx.x; u32 spins=0;
#define pq ((const char*)A.wq+((size_t)(w&1)*224*4096+h*256+(w>>1)*128)*2)
#define po ((const char*)A.wo+((size_t)h*256*448+w*112)*2)
#define xn (smem+L_XN+w*1792)
#define FRESH do{ lane=lane_id(); asm volatile("" : "+s"(h),"+s"(w)); }while(0)
#define g (lane>>4)
#define n (lane&15)
#define n14 (n<14?n:13)
#define tid (w*64+lane)
#define voq (u32)((g*8*4096+n*8)*2)
#define vo4 (u32)((g*8*448+n14*8)*2)
  f32x4 d[8];
  ldwn<true,0,56>(voq,pq,8192);
  { int lc=lane<56?lane:55;
    #pragma unroll
    for(int b=0;b<2;b++){ u32x4 v=*(const u32x4*)(A.x+b*448+lc*8); float x[8];
      #pragma unroll
      for(int i=0;i<4;i++){ x[2*i]=__uint_as_float(v[i]<<16); x[2*i+1]=__uint_as_float(v[i]&0xffff0000u); }
      lnorm(x,lane,xn+b*896);
      if(w==0&&lane<56){ LDSP f32x4* r=(LDSP f32x4*)(smem+L_XR+b*1792)+lane*2; r[0]=f32x4{x[0],x[1],x[2],x[3]}; r[1]=f32x4{x[4],x[5],x[6],x[7]}; } } }
  zero_p(ws,0,h,tid); zero_p(ws,1,h,tid);
  #pragma nounroll
  for(int s=-1;s<4;s++){
    float x[8]; const int b=s&1; FRESH;
    if(s>=0){
      {
        u32x4 q=pollq(ws,s,h,spins); St st; st_reset(st); u32 pp[4]; u32x4 r[8];
        size_t off=(((size_t)b*16+h)*16384+16320+w*16)*512+lane*16;
        #pragma unroll
        for(int i=0;i<8;i++) r[i]=__builtin_nontemporal_load((const u32x4*)(A.K+off+i*1024));
        float a[4];
        #pragma unroll
        for(int i=0;i<4;i++) a[i]=swp16(kdot(r[2*i].x,r[2*i].y,r[2*i].z,r[2*i].w,q),kdot(r[2*i+1].x,r[2*i+1].y,r[2*i+1].z,r[2*i+1].w,q));
        asm volatile("":::"memory");
        #pragma unroll
        for(int i=0;i<8;i++) r[i]=__builtin_nontemporal_load((const u32x4*)(A.V+off+i*1024));
        spread(soft(a[0],a[1],a[2],a[3],lane,st),pp);
        #pragma unroll
        for(int j=0;j<4;j++){ u32 t[8]={r[j].x,r[j].y,r[j].z,r[j].w,r[j+4].x,r[j+4].y,r[j+4].z,r[j+4].w}; pvr(t,t+4,pp[j],st); }
        publish(ws,st,s,h,60+w);
      }
      FRESH;
      {
        u32x2 ml; do{ ml=__builtin_amdgcn_raw_buffer_load_b64(ws,lane*8,WS_ML+(s*16+h)*512,16); } while(retry(ml.x&ml.y,spins));
        float m=__uint_as_float(ml.x), M=wmax(m), f=__builtin_amdgcn_exp2f(m-M), L=wsum(f*__uint_as_float(ml.y)); f=f/L;
        f32x4 o={0.f,0.f,0.f,0.f};
        #pragma nounroll
        for(int hf=0;hf<2;hf++){ u32x4 v[8]; u32 ok; u32 so=WS_P+((s*16+h)*64+w*16+hf*8)*1024;
          do{ ok=~0u;
            #pragma unroll
            for(int i=0;i<8;i++){ v[i]=ld16(ws,lane*16+(i&3)*1024,so+(i>>2)*4096); ok&=and4(v[i]); }
          } while(retry(ok,spins));
          #pragma unroll
          for(int i=0;i<8;i++) o+=__builtin_bit_cast(f32x4,v[i])*__int_as_float(__builtin_amdgcn_readlane(__float_as_int(f),w*16+hf*8+i));
        }
        LDSP f32x4* r=(LDSP f32x4*)(smem+L_RED2)+lane; r[w*64]=o; __syncthreads();
        o=(r[0]+r[64])+(r[128]+r[192]);
        *(LDSP u32x2*)(xn+(lane&31)*16+(lane>>5)*8)=u32x2{pk2(o.x,o.y),pk2(o.z,o.w)};
      }
      FRESH; zero8(d); gemv<true,0,8>(d,(const LDSP u32x4*)xn+g);
      { f32x4 y=pick4(d,g&1); if(g<2&&n<14) st16(ws,(n*8+g*4)*4,tag4(y.x,y.y,y.z,y.w),WS_Y+((s*2+0)*16+h)*1792+w*448); }
      if(s<2) ldwn<true,0,56>(voq,pq,8192);
      gather_y(ws,WS_Y+(s*2+0)*16*1792,smem,b,w,x,spins);
      FRESH; lnorm(x,lane,xn); FRESH;
      zero8(d); { const LDSP u32x4* xa=(const LDSP u32x4*)xn+(w&1)*28+g; gemv<false,64,6>(d,xa); u32x4 a=xa[24]; const LDSP u32x4* wl=(const LDSP u32x4*)(smem+L_GU+w*7168)+g*14+n14;
        #pragma unroll
        for(int e=0;e<8;e+=2) mf2(d[e],d[e+1],a,wl[e*56],wl[e*56+56]); }
      { f32x4 y=pick4(d,g&1); if(g<2) *(LDSP f32x4*)(smem+L_RED3+w*1024+(n*8+g*4)*4)=y; }
      __syncthreads();
      { const LDSP float* r=(const LDSP float*)(smem+L_RED3)+lane*2; float hv[2];
        #pragma unroll
        for(int i=0;i<2;i++){ float gg=rbf(r[i]+r[256+i]), uu=rbf(r[512+i]+r[768+i]); hv[i]=rbf(1.f/(1.f+__expf(-gg)))*uu; }
        *(LDSP u32*)(smem+L_HV+w*256+lane*4)=lane<56?pk2(hv[0],hv[1]):0u; }
      zero8(d); FRESH;
      { const LDSP u32x4* xa=(const LDSP u32x4*)(smem+L_HV+w*256)+g; const LDSP u32x4* wl=(const LDSP u32x4*)(smem+L_WD+w*25088);
        #pragma unroll
        for(int kb=0;kb<4;kb++){ u32x4 a=xa[kb*4];
          #pragma unroll
          for(int e=0;e<8;e+=2){ const LDSP u32x4* p=kb<3?wl+(kb*448+e*56+g*14+n14):wl+(1344+e*28+(g&1)*14+n14); mf2(d[e],d[e+1],a,p[0],p[kb<3?56:28]); } } }
      { f32x4 y=pick4(d,g&1); if(g<2&&n<14) st16(ws,(n*8+g*4)*4,tag4(y.x,y.y,y.z,y.w),WS_Y+((s*2+1)*16+h)*1792+w*448); }
      FRESH;
      if(s<2||h==0){
        gather_y(ws,WS_Y+(s*2+1)*16*1792,smem,b,w,x,spins); FRESH;
        if(s>=2&&w==0&&lane<56) *(u32x4*)(A.out+b*448+lane*8)=u32x4{pk2(x[0],x[1]),pk2(x[2],x[3]),pk2(x[4],x[5]),pk2(x[6],x[7])};
        lnorm(x,lane,xn);
      }
    }
    if(s>=2) continue;
    WAITV(0); FRESH;
    zero8(d); gemv_raw<0,7>(d,(const LDSP u32x4*)(xn+((s<0&&n>=8)?896:0))+(w&1)*28+g);
    { f32x4 y=pick4(d,g&1); *(LDSP f32x4*)(smem+L_RED2+w*1024+(g>>1)*512+(n*8+(g&1)*4)*4)=y; }
    __syncthreads();
    if(w==0||(w==1&&s<0)){ const LDSP f32x4* r=(const LDSP f32x4*)(smem+L_RED2+(lane>>5)*2048+w*512)+(lane&31); f32x4 y=r[0]+r[64];
      st16(ws,lane*16,tag4(rbf(y.x),rbf(y.y),rbf(y.z),rbf(y.w)),WS_Q+((s<0?w:s+2)*16+h)*1024); }
    FRESH; ldwn<true,0,32>(vo4,po,896);
    if(s<0){
      const char* pg=(const char*)((w>>1)?A.wu:A.wg)+((size_t)(w&1)*224*1792+h*112)*2; const u32 vog=(g*8*1792+n14*8)*2;
      kb_to_lds(pg+(size_t)192*3584,vog,3584,smem+L_GU+w*7168+(g*14+n14)*16,896,n<14);
      ldwn<false,64,48>(vog,pg,3584); FRESH;
      const char* pd=(const char*)A.wd+((size_t)h*112*448+w*112)*2;
      #pragma nounroll
      for(int kb=0;kb<3;kb++) kb_to_lds(pd+(size_t)kb*32*896,vo4,896,smem+L_WD+w*25088+kb*7168+(g*14+n14)*16,896,n<14);
      FRESH; kb_to_lds(pd+(size_t)96*896,((g&1)*8*448+n14*8)*2,896,smem+L_WD+w*25088+21504+((g&1)*14+n14)*16,448,n<14&&g<2);
      FRESH; zero_p(ws,2,h,tid); zero_p(ws,3,h,tid);
      WAITV(0); permute<false,64,6>();
    }
    FRESH; ldwn<true,128,32>(vo4,po+(size_t)128*896,896);
    WAITV(0); permute<true,0,8>();
    __syncthreads();
  }
}
#undef g
#undef n
#undef n14
#undef tid
#undef xn
__global__ __attribute__((amdgpu_num_vgpr(64))) __launch_bounds__(256,1) void fused(Args A){
  extern __shared__ __attribute__((aligned(16))) char smem_[];
  asm volatile("" ::: "v255","a255");
  LDSP char* smem=(LDSP char*)smem_;
  const int lane=threadIdx.x&63, w=__builtin_amdgcn_readfirstlane(threadIdx.x>>6);
  rsrc_t ws=__builtin_amdgcn_make_buffer_rsrc(A.ws,0,WS_END,0x00020000);
  if(blockIdx.x<16) chain(A,ws,smem,lane,w); else streamer(A,ws,smem,lane,w);
}
extern "C" void kernel_launch(void* const* d_in,const int* in_sizes,int n_in,void* d_out,int out_size,void* d_ws,size_t ws_size,hipStream_t stream){
  Args A; A.x=(const u16*)d_in[0]; A.K=(const char*)d_in[1]; A.V=(const char*)d_in[2]; A.wq=(const u16*)d_in[3]; A.wo=(const u16*)d_in[4];
  A.wg=(const u16*)d_in[6]; A.wu=(const u16*)d_in[7]; A.wd=(const u16*)d_in[8]; A.ws=(char*)d_ws; A.out=(u16*)d_out;
  (void)hipMemsetAsync(d_ws,0,WS_ZEND,stream);
  (void)hipFuncSetAttribute((const void*)fused,hipFuncAttributeMaxDynamicSharedMemorySize,163840);
  fused<<<256,256,163840,stream>>>(A);
}
```

```cpp
#include <hip/hip_runtime.h>
#include <hip/hip_bf16.h>
typedef unsigned u32;
typedef unsigned short u16;
typedef u32 u32x4 __attribute__((ext_vector_type(4)));
typedef u32 u32x2 __attribute__((ext_vector_type(2)));
typedef float f32x4 __attribute__((ext_vector_type(4)));
typedef __bf16 bf16x2 __attribute__((ext_vector_type(2)));
typedef __bf16 bf16x8 __attribute__((ext_vector_type(8)));
typedef __amdgpu_buffer_rsrc_t rsrc_t;
#define DEV __device__ __forceinline__
#define LDSP __attribute__((address_space(3)))
#define PR "nt"
#define NKEEP 9
#define NKK 0
#define PKEEP ""
#define PS1 "nt"
constexpr u32 WS_Q = 0;
constexpr u32 WS_Y = 65536;
constexpr u32 WS_EP = 0x70000;
constexpr u32 WS_Z = 0x78000;
constexpr u32 WS_ML = 0x80000;
constexpr u32 WS_P = 0x100000;
constexpr u32 WS_END = WS_P + 4*16*64*1024;
constexpr int RING = 64, VR0 = 96;
constexpr u32 SPIN_MAX = 1u << 20;

DEV float bf2f(u16 v){ return __uint_as_float((u32)v<<16); }
DEV float rbf(float x){ return (float)(__bf16)x; }
DEV float dot2(u32 a,u32 b,float c){ return __builtin_amdgcn_fdot2_f32_bf16(__builtin_bit_cast(bf16x2,a),__builtin_bit_cast(bf16x2,b),c,false); }
DEV u32 pk2(float a,float b){ bf16x2 v; v.x=(__bf16)a; v.y=(__bf16)b; return __builtin_bit_cast(u32,v); }
DEV u32 plo(u32 a,u32 b){ return __builtin_amdgcn_perm(b,a,0x05040100); }
DEV u32 phi(u32 a,u32 b){ return __builtin_amdgcn_perm(b,a,0x07060302); }
template<int CTRL> DEV float dpp(float x){ return __int_as_float(__builtin_amdgcn_update_dpp(0,__float_as_int(x),CTRL,0xf,0xf,true)); }
DEV float kdot(u32 k0,u32 k1,u32 k2,u32 k3,u32x4 q){ return dot2(k3,q.w,dot2(k2,q.z,dot2(k1,q.y,dot2(k0,q.x,0.f)))); }
DEV float swp16(float a,float b){ auto r=__builtin_amdgcn_permlane16_swap(__float_as_uint(a),__float_as_uint(b),false,false); return __uint_as_float(r[0])+__uint_as_float(r[1]); }
DEV float swp32(float a,float b){ auto r=__builtin_amdgcn_permlane32_swap(__float_as_uint(a),__float_as_uint(b),false,false); return __uint_as_float(r[0])+__uint_as_float(r[1]); }
DEV float wsum(float v){ v+=dpp<0xb1>(v); v+=dpp<0x4e>(v); v+=dpp<0x141>(v); v+=dpp<0x128>(v); v=swp16(v,v); return swp32(v,v); }
DEV float wmax(float v){
  v=fmaxf(v,dpp<0xb1>(v)); v=fmaxf(v,dpp<0x4e>(v)); v=fmaxf(v,dpp<0x141>(v)); v=fmaxf(v,dpp<0x128>(v));
  auto r=__builtin_amdgcn_permlane16_swap(__float_as_uint(v),__float_as_uint(v),false,false); v=fmaxf(__uint_as_float(r[0]),__uint_as_float(r[1]));
  r=__builtin_amdgcn_permlane32_swap(__float_as_uint(v),__float_as_uint(v),false,false); return fmaxf(__uint_as_float(r[0]),__uint_as_float(r[1]));
}
struct St{ float m,l,acc[8]; };
DEV void st_reset(St& s){ s.m=-1e30f; s.l=0.f;
  #pragma unroll
  for(int i=0;i<8;i++) s.acc[i]=0.f; }
DEV int lane_id(){ int l; asm volatile("v_mbcnt_lo_u32_b32 %0, -1, 0\n\tv_mbcnt_hi_u32_b32 %0, -1, %0" : "=v"(l)); return l; }
DEV float uni(float x){ return __int_as_float(__builtin_amdgcn_readfirstlane(__float_as_int(x))); }
DEV u32 soft(float a0,float a1,float a2,float a3,int lane,St& st){
  a0+=dpp<0x128>(a0); a1+=dpp<0x128>(a1); a2+=dpp<0x128>(a2); a3+=dpp<0x128>(a3);
  float b0=(lane&8)?a1:a0, b1=(lane&8)?a3:a2;
  b0+=dpp<0x141>(b0); b1+=dpp<0x141>(b1);
  float c=(lane&4)?b1:b0;
  c+=dpp<0xb1>(c); c+=dpp<0x4e>(c);
  float s=rbf(c)*(0.0625f*1.4426950408889634f);
  if(__builtin_expect(__any(s>st.m),0)){
    float mn=uni(fmaxf(st.m,wmax(s))); float f=__builtin_amdgcn_exp2f(st.m-mn); st.m=mn; st.l*=f;
    #pragma unroll
    for(int i=0;i<8;i++) st.acc[i]*=f; }
  float p=__builtin_amdgcn_exp2f(s-st.m); st.l+=p;
  return pk2(p,dpp<0x141>(p));
}
DEV void spread(u32 pr,u32* pp){
  pp[0]=__builtin_amdgcn_ds_swizzle(pr,0<<5); pp[1]=__builtin_amdgcn_ds_swizzle(pr,16<<5);
  pp[2]=__builtin_amdgcn_ds_swizzle(pr,8<<5); pp[3]=__builtin_amdgcn_ds_swizzle(pr,24<<5);
}
DEV void pin(St& s){ asm volatile("" : "+v"(s.acc[0]),"+v"(s.acc[1]),"+v"(s.acc[2]),"+v"(s.acc[3]),"+v"(s.acc[4]),"+v"(s.acc[5]),"+v"(s.acc[6]),"+v"(s.acc[7])); }
DEV float pinf(float x){ asm volatile("" : "+v"(x)); return x; }
DEV void pvr(const u32* a,const u32* b,u32 pp,St& st){
  #pragma unroll
  for(int c=0;c<4;c++){ st.acc[2*c]=dot2(plo(a[c],b[c]),pp,st.acc[2*c]); st.acc[2*c+1]=dot2(phi(a[c],b[c]),pp,st.acc[2*c+1]); }
  pin(st);
}
template<int N> DEV void waitv(){ __builtin_amdgcn_sched_barrier(0); asm volatile("s_waitcnt vmcnt(%0)" :: "n"(N) : "memory"); }
#define WAITV(n) do{ __builtin_amdgcn_sched_barrier(0); asm volatile("s_waitcnt vmcnt(" #n ")" ::: "memory"); }while(0)
template<int R> DEV void rdA8(u32* t,int){ asm volatile(
  "v_accvgpr_read_b32 %0, a[%8]\n\tv_accvgpr_read_b32 %1, a[%8+1]\n\tv_accvgpr_read_b32 %2, a[%8+2]\n\tv_accvgpr_read_b32 %3, a[%8+3]\n\t"
  "v_accvgpr_read_b32 %4, a[%8+4]\n\tv_accvgpr_read_b32 %5, a[%8+5]\n\tv_accvgpr_read_b32 %6, a[%8+6]\n\tv_accvgpr_read_b32 %7, a[%8+7]"
  : "=v"(t[0]),"=v"(t[1]),"=v"(t[2]),"=v"(t[3]),"=v"(t[4]),"=v"(t[5]),"=v"(t[6]),"=v"(t[7]) : "n"(R)); }
template<int R> DEV float dotV2(u32x4 q){ float s0,s1; asm volatile(
  "v_dot2_f32_bf16 %0, v[%6], %2, 0\n\tv_dot2_f32_bf16 %1, v[%6+4], %2, 0\n\tv_dot2_f32_bf16 %0, v[%6+1], %3, %0\n\tv_dot2_f32_bf16 %1, v[%6+5], %3, %1\n\t"
  "v_dot2_f32_bf16 %0, v[%6+2], %4, %0\n\tv_dot2_f32_bf16 %1, v[%6+6], %4, %1\n\tv_dot2_f32_bf16 %0, v[%6+3], %5, %0\n\tv_dot2_f32_bf16 %1, v[%6+7], %5, %1\n\ts_nop 2"
  : "=&v"(s0),"=&v"(s1) : "v"(q.x),"v"(q.y),"v"(q.z),"v"(q.w),"n"(R)); return swp16(s0,s1); }
template<int R> DEV void permV2(u32* t){ asm volatile(
  "v_perm_b32 %0, v[%10+4], v[%10], %8\n\tv_perm_b32 %1, v[%10+4], v[%10], %9\n\tv_perm_b32 %2, v[%10+5], v[%10+1], %8\n\tv_perm_b32 %3, v[%10+5], v[%10+1], %9\n\t"
  "v_perm_b32 %4, v[%10+6], v[%10+2], %8\n\tv_perm_b32 %5, v[%10+6], v[%10+2], %9\n\tv_perm_b32 %6, v[%10+7], v[%10+3], %8\n\tv_perm_b32 %7, v[%10+7], v[%10+3], %9"
  : "=v"(t[0]),"=v"(t[1]),"=v"(t[2]),"=v"(t[3]),"=v"(t[4]),"=v"(t[5]),"=v"(t[6]),"=v"(t[7]) : "s"(0x05040100u),"s"(0x07060302u),"n"(R)); }
DEV void pvt(const u32* t,u32 pp,St& st){
  #pragma unroll
  for(int c=0;c<8;c++) st.acc[c]=dot2(t[c],pp,st.acc[c]);
  pin(st); }
#define LD2(F,POL) asm volatile("global_load_dwordx4 " F "[%2:%2+3], %0, %1 offset:%3 " POL "\n\tglobal_load_dwordx4 " F "[%2+4:%2+7], %0, %1 offset:%4 " POL \
  :: "v"(vo),"s"(mid),"n"(R),"n"(O0),"n"(O1) : "memory")
template<int R,int O0,int O1> DEV void ldV2r(u32 vo,const char* mid){ LD2("v",PR); }
template<int R,int O0,int O1> DEV void ldA2r(u32 vo,const char* mid){ LD2("a",PR); }
template<int R,int O0,int O1> DEV void ldV2s(u32 vo,const char* mid){ LD2("v",PS1); }
template<int R,int O0,int O1> DEV void ldV2k(u32 vo,const char* mid,bool keep){ if(keep) LD2("v",PKEEP); else LD2("v",PS1); }
template<bool ISV,int I> struct Off{ static constexpr int a=(ISV?I:2*I)*1024-4096, b=(ISV?I+4:2*I+1)*1024-4096; };
template<int G> DEV void issue(u32 vo,const char* mid,u32 ldsw){
  constexpr bool V=G&1;
  if constexpr(G<8){ constexpr int R=32*G; ldA2r<R,Off<V,0>::a,Off<V,0>::b>(vo,mid); ldA2r<R+8,Off<V,1>::a,Off<V,1>::b>(vo,mid); ldA2r<R+16,Off<V,2>::a,Off<V,2>::b>(vo,mid); ldA2r<R+24,Off<V,3>::a,Off<V,3>::b>(vo,mid); }
  else if constexpr(G<13){ constexpr int R=VR0+32*(G-8); ldV2r<R,Off<V,0>::a,Off<V,0>::b>(vo,mid); ldV2r<R+8,Off<V,1>::a,Off<V,1>::b>(vo,mid); ldV2r<R+16,Off<V,2>::a,Off<V,2>::b>(vo,mid); ldV2r<R+24,Off<V,3>::a,Off<V,3>::b>(vo,mid); }
  else { u32 m0a=ldsw+(G-13)*8192; const char* lo=mid-4096;
    asm volatile("s_waitcnt lgkmcnt(0)\n\ts_mov_b32 m0, %3\n\ts_nop 0\n\t"
      "global_load_lds_dwordx4 %0, %1 " PR "\n\tglobal_load_lds_dwordx4 %0, %1 offset:1024 " PR "\n\tglobal_load_lds_dwordx4 %0, %1 offset:2048 " PR "\n\tglobal_load_lds_dwordx4 %0, %1 offset:3072 " PR "\n\t"
      "s_mov_b32 m0, %4\n\ts_nop 0\n\t"
      "global_load_lds_dwordx4 %0, %2 " PR "\n\tglobal_load_lds_dwordx4 %0, %2 offset:1024 " PR "\n\tglobal_load_lds_dwordx4 %0, %2 offset:2048 " PR "\n\tglobal_load_lds_dwordx4 %0, %2 offset:3072 " PR
      :: "v"(vo),"s"(lo),"s"(mid),"s"(m0a),"s"(m0a+4096) : "memory"); }
}
#define SB __builtin_amdgcn_sched_barrier(0)
template<int G,int I> DEV float kpair(u32x4 q,const LDSP u32x4* lw){ SB;
  if constexpr(G<8){ u32 t[8]; rdA8<32*G+8*I>(t,0); return pinf(swp16(kdot(t[0],t[1],t[2],t[3],q),kdot(t[4],t[5],t[6],t[7],q))); }
  else if constexpr(G<13) return dotV2<VR0+32*(G-8)+8*I>(q);
  else { u32x4 a=lw[(G-13)*512+(2*I)*64], b=lw[(G-13)*512+(2*I+1)*64]; return pinf(swp16(kdot(a.x,a.y,a.z,a.w,q),kdot(b.x,b.y,b.z,b.w,q))); }
}
template<int G,int I> DEV void vpair(u32 pp,St& st,const LDSP u32x4* lw){
  u32 t[8]; SB;
  if constexpr(G<8){ rdA8<32*G+8*I>(t,0); pvr(t,t+4,pp,st); }
  else if constexpr(G<13){ permV2<VR0+32*(G-8)+8*I>(t); pvt(t,pp,st); }
  else { u32x4 a=lw[(G-13)*512+I*64], b=lw[(G-13)*512+(I+4)*64]; t[0]=a.x;t[1]=a.y;t[2]=a.z;t[3]=a.w;t[4]=b.x;t[5]=b.y;t[6]=b.z;t[7]=b.w; pvr(t,t+4,pp,st); }
}
template<int G> DEV void comp(u32x4 q,St& st,u32& pr,int lane,const LDSP u32x4* lw){
  if constexpr(!(G&1)){ float a0=kpair<G,0>(q,lw),a1=kpair<G,1>(q,lw),a2=kpair<G,2>(q,lw),a3=kpair<G,3>(q,lw); pr=soft(a0,a1,a2,a3,lane,st); asm volatile("" : "+v"(pr)); }
  else { u32 pp[4]; asm volatile("" : "+v"(pr)); spread(pr,pp); vpair<G,0>(pp[0],st,lw); vpair<G,1>(pp[1],st,lw); vpair<G,2>(pp[2],st,lw); vpair<G,3>(pp[3],st,lw); }
}
DEV void ringK(u32 vo,const char* mid){ ldV2s<RING,-4096,-3072>(vo,mid); ldV2s<RING+8,-2048,-1024>(vo,mid); ldV2s<RING+16,0,1024>(vo,mid); ldV2s<RING+24,2048,3072>(vo,mid); }
struct Cx{ u32x4 qO,qN; St sO,sN; u32 ppO,ppN; const char *kn,*vn,*ringnext; int it,lane; u32 vo,ldsw,rot; const LDSP u32x4* lw; };
#define ROTF (u32)(h+slot)
#define LAG 2
#define PREN 14
constexpr int PRE = PREN;
template<int G> DEV const char* gmid(const Cx& c){ u32 o=c.rot; asm volatile("" : "+s"(o)); o+=(G>>1)*8192; if(o>=9*8192) o-=9*8192; return ((G&1)?c.vn:c.kn)+(o+4096); }
template<int G> DEV void rstep(Cx& c){
  if(c.it==0){
    if constexpr(G+PRE<18) issue<G+PRE>(c.vo,gmid<G+PRE>(c),c.ldsw);
    if constexpr(G+PRE==18) ringK(c.vo,c.ringnext);
    if constexpr(G-LAG>=PRE) waitv<(18-(G-LAG))*8>();
  } else if constexpr(G<18){
    comp<G>(c.qO,c.sO,c.ppO,c.lane,c.lw);
    issue<G>(c.vo,gmid<G>(c),c.ldsw); waitv<8*LAG>();
  } else waitv<(17-(G-LAG))*8>();
  if constexpr(G>=LAG) comp<G-LAG>(c.qN,c.sN,c.ppN,c.lane,c.lw);
  if constexpr(G<17+LAG) rstep<G+1>(c);
}
template<int G> DEV void prefill(const Cx& c){ if constexpr(G<PRE){ issue<G>(c.vo,gmid<G>(c),c.ldsw); if constexpr(G>0) WAITV(16); prefill<G+1>(c); } }
DEV u32x4 ld16(rsrc_t r,u32 off,u32 so=0){ return __builtin_amdgcn_raw_buffer_load_b128(r,off,so,16); }
DEV bool tryq(rsrc_t ws,u32 so,u32 tag,u32x4& q){
  u32 off=(lane_id()&31)*32; u32x4 a=ld16(ws,off,so), b=ld16(ws,off+16,so); q=u32x4{a.x,a.z,b.x,b.z};
  return __all(a.y==tag&&a.w==tag&&b.y==tag&&b.w==tag);
}
DEV bool stream(Cx& c,const char* kmid,const char* vmid,const char* nextk,bool has_next,rsrc_t ws,u32 qso,u32 tag){
  bool got=false;
  #pragma nounroll
  for(int t=0;t<9;t++){
    if(t==8&&qso) got=tryq(ws,qso,tag,c.qO);
    const bool keep=has_next&&t>=9-NKEEP;
    WAITV(6); float a0=dotV2<RING>(c.qN);    ldV2k<RING,   Off<1,0>::a,Off<1,0>::b>(c.vo,vmid,keep);
    WAITV(6); float a1=dotV2<RING+8>(c.qN);  ldV2k<RING+8, Off<1,1>::a,Off<1,1>::b>(c.vo,vmid,keep);
    WAITV(6); float a2=dotV2<RING+16>(c.qN); ldV2k<RING+16,Off<1,2>::a,Off<1,2>::b>(c.vo,vmid,keep);
    WAITV(6); float a3=dotV2<RING+24>(c.qN); ldV2k<RING+24,Off<1,3>::a,Off<1,3>::b>(c.vo,vmid,keep);
    u32 pp[4]; spread(soft(a0,a1,a2,a3,c.lane,c.sN),pp);
    { int d=(t*8192+c.rot==8*8192)?-8*8192:8192; kmid+=d; vmid+=d; } if(t==8) kmid=nextk;
    const bool keepk=has_next&&qso&&t<NKK;
    bool pf=(t<8)||has_next; u32 x[8];
    if(!pf) WAITV(0);
    WAITV(6); permV2<RING>(x);    if(pf) ldV2k<RING,-4096,-3072>(c.vo,kmid,keepk);   pvt(x,pp[0],c.sN);
    WAITV(6); permV2<RING+8>(x);  if(pf) ldV2k<RING+8,-2048,-1024>(c.vo,kmid,keepk); pvt(x,pp[1],c.sN);
    WAITV(6); permV2<RING+16>(x); if(pf) ldV2k<RING+16,0,1024>(c.vo,kmid,keepk);     pvt(x,pp[2],c.sN);
    WAITV(6); permV2<RING+24>(x); if(pf) ldV2k<RING+24,2048,3072>(c.vo,kmid,keepk);  pvt(x,pp[3],c.sN);
  }
  return got;
}
DEV void st16(rsrc_t r,u32 off,u32x4 v,u32 so=0){ __builtin_amdgcn_raw_buffer_store_b128(v,r,off,so,16); }
DEV u32x4 tag4(float a,float b,float c,float d){ u32x4 v={__float_as_uint(a)|1u,__float_as_uint(b)|1u,__float_as_uint(c)|1u,__float_as_uint(d)|1u}; return v; }
DEV u32 and4(u32x4 v){ return v.x&v.y&v.z&v.w; }
DEV bool retry(u32 ok,u32& spins){ if(__all(ok&1u)) return false; if(++spins>SPIN_MAX) return false; __builtin_amdgcn_s_sleep(2); asm volatile("":::"memory"); return true; }
DEV void pollq(rsrc_t ws,u32 so,u32 tag,u32x4& q,u32& spins){ while(retry(tryq(ws,so,tag,q),spins)){} }
DEV u32 launch_tag(const char* ws){ return *(const __attribute__((address_space(4))) u32*)(size_t)(ws+WS_EP)+1u; }
DEV void publish(rsrc_t ws,const St& s,int step,int h,int slot){
  u32 idx=(step*16+h)*64+slot;
  int lane=lane_id();
  st16(ws,lane*16,tag4(swp32(s.acc[0],s.acc[4]),swp32(s.acc[1],s.acc[5]),swp32(s.acc[2],s.acc[6]),swp32(s.acc[3],s.acc[7])),WS_P+idx*1024);
  float l=wsum(s.l)*0.25f;
  if(lane==0){ u32x2 v={__float_as_uint(s.m)|1u,__float_as_uint(l)|1u}; __builtin_amdgcn_raw_buffer_store_b64(v,ws,0,WS_ML+idx*8,16); }
}
DEV void zero_slots(rsrc_t ws,int h,int slot){
  int lane=lane_id(); u32 z; asm volatile("v_mov_b32 %0, 0" : "=v"(z));
  #pragma unroll
  for(int s=0;s<4;s++) st16(ws,lane*16,u32x4{z,z,z,z},WS_P+((s*16+h)*64+slot)*1024);
  if(lane<4) __builtin_amdgcn_raw_buffer_store_b64(u32x2{z,z},ws,lane*8192,WS_ML+(h*64+slot)*8,16);
}
DEV void mark_zeroed(rsrc_t ws,int h,int slot,u32 tag){ asm volatile("" : "+s"(tag)); if(lane_id()==0) __builtin_amdgcn_raw_buffer_store_b64(u32x2{tag,~tag},ws,0,WS_Z+(h*64+slot)*8,16); }
#define TS(i)
struct Args{ const u16 *x,*wq,*wo,*wg,*wu,*wd; const char *K,*V; char* ws; u16* out; };
template<class T> DEV void mv(T& d,T s){ asm volatile("v_mov_b32 %0, %1" : "+v"(d) : "v"(s)); }
DEV void mvq(Cx& c){ u32 a=c.qN.x,b=c.qN.y,d=c.qN.z,e=c.qN.w; mv(a,(u32)c.qO.x); mv(b,(u32)c.qO.y); mv(d,(u32)c.qO.z); mv(e,(u32)c.qO.w); c.qN=u32x4{a,b,d,e}; }
DEV void clr(St& s){ s.m=-1e30f; asm volatile("v_mov_b32 %0, 0" : "+v"(s.l));
  #pragma unroll
  for(int i=0;i<8;i++) asm volatile("v_mov_b32 %0, 0" : "+v"(s.acc[i])); }
DEV void streamer(const Args& A,rsrc_t ws,LDSP char* smem,int lane,int w){
  const int idx=blockIdx.x-32, h=idx&15, slot=(idx>>4)*4+w;
  const size_t BS=(size_t)16*16384*512, off=((size_t)h*16384+slot*288)*512;
  const char *k0=A.K+off,*v0=A.V+off;
  Cx c; c.rot=__builtin_amdgcn_readfirstlane(ROTF%9*8192); const u32 S=9*8192+4096+c.rot; c.lane=lane; c.vo=lane*16; c.ldsw=(u32)(size_t)smem+w*40960; c.lw=(const LDSP u32x4*)(smem+w*40960)+lane;
  u32 spins=0; TS(0);
  c.kn=k0; c.vn=v0; c.ringnext=k0+S;
  issue<0>(c.vo,gmid<0>(c),0); issue<1>(c.vo,gmid<1>(c),0); zero_slots(ws,h,slot); const u32 tag=launch_tag(A.ws), qb=WS_Q+h*1024;
  waitv<13>(); issue<2>(c.vo,gmid<2>(c),0); waitv<13>(); prefill<3>(c);
  TS(1); while(retry(tryq(ws,qb,tag,c.qN)&tryq(ws,qb+16384,tag,c.qO),spins)){} TS(2);
  WAITV(0); mark_zeroed(ws,h,slot,tag);
  st_reset(c.sN); st_reset(c.sO); c.ppO=0; c.ppN=0;
  #pragma nounroll
  for(int ph=0;;ph++){
    if(ph!=1){ c.it=ph-(ph>0); rstep<0>(c); TS(3+c.it*4); }
    if(ph==3) break;
    if(ph==2){ publish(ws,c.sN,1,h,slot); c.sN.m=c.sO.m; mv(c.sN.l,c.sO.l); mvq(c);
      #pragma unroll
      for(int i=0;i<8;i++) mv(c.sN.acc[i],c.sO.acc[i]);
      clr(c.sO); }
    const size_t o=(ph==1?BS:0)+S; const u32 qso=ph?qb+(ph+1)*16384:0;
    bool got=stream(c,k0+o,v0+o,k0+(ph==0?BS:0)+S,ph<2,ws,qso,tag);
    if(ph!=1){ publish(ws,c.sN,ph,h,slot); clr(c.sN); }
    TS(ph==2?8:4+ph);
    if(ph&&!got) pollq(ws,qso,tag,c.qO,spins);
    TS(ph==2?9:6);
    if(ph!=1) mvq(c);
    c.kn=k0+BS+(ph==2?9*8192:0); c.vn=v0+BS+(ph==2?9*8192:0);
  }
  float m=fmaxf(c.sN.m,c.sO.m), fn=__builtin_amdgcn_exp2f(c.sN.m-m), fo=__builtin_amdgcn_exp2f(c.sO.m-m);
  c.sN.m=m; c.sN.l=pinf(c.sN.l*fn+c.sO.l*fo);
  #pragma unroll
  for(int i=0;i<8;i++) asm volatile("v_mul_f32 %0, %0, %2\n\tv_fmac_f32 %0, %1, %3" : "+v"(c.sN.acc[i]) : "v"(c.sO.acc[i]),"v"(fn),"v"(fo));
  publish(ws,c.sN,3,h,slot); TS(12);
}
#define PW ""
constexpr int L_WQ=0  , L_RED=32768  , L_XN=40960  , L_RED2=49152  , L_RED3=53248  , L_OV=57344  , L_HV=57856  , L_XR=58880  , L_QR=62464  ;
template<int AG,int R> DEV void ldw1(u32 vo,const char* p){
  if constexpr(AG==2) asm volatile("global_load_dwordx4 a[%2:%2+3], %0, %1" :: "v"(vo),"s"(p),"n"(R) : "memory");
  else if constexpr(AG==1) asm volatile("global_load_dwordx4 a[%2:%2+3], %0, %1 " PW :: "v"(vo),"s"(p),"n"(R) : "memory");
  else asm volatile("global_load_dwordx4 v[%2:%2+3], %0, %1 " PW :: "v"(vo),"s"(p),"n"(R) : "memory"); }
template<int AG,int R,int N,int I=0> DEV void ldwn(u32 vo,const char* p,int rowb){ if constexpr(I<N){ asm volatile("" : "+s"(p)); ldw1<AG,R+4*I>(vo,p); ldwn<AG,R,N,I+1>(vo,p+((I&7)==7?25:1)*rowb,rowb); } }
template<int R> DEV void rdV8(u32* t){ asm volatile(
  "v_mov_b32 %0, v[%8]\n\tv_mov_b32 %1, v[%8+1]\n\tv_mov_b32 %2, v[%8+2]\n\tv_mov_b32 %3, v[%8+3]\n\tv_mov_b32 %4, v[%8+4]\n\tv_mov_b32 %5, v[%8+5]\n\tv_mov_b32 %6, v[%8+6]\n\tv_mov_b32 %7, v[%8+7]"
  : "=v"(t[0]),"=v"(t[1]),"=v"(t[2]),"=v"(t[3]),"=v"(t[4]),"=v"(t[5]),"=v"(t[6]),"=v"(t[7]) : "n"(R)); }
template<bool AG,int R> DEV void wr4(u32x4 v){
  if constexpr(AG) asm volatile("v_accvgpr_write_b32 a[%4], %0\n\tv_accvgpr_write_b32 a[%4+1], %1\n\tv_accvgpr_write_b32 a[%4+2], %2\n\tv_accvgpr_write_b32 a[%4+3], %3" :: "v"(v.x),"v"(v.y),"v"(v.z),"v"(v.w),"n"(R));
  else asm volatile("v_mov_b32 v[%4], %0\n\tv_mov_b32 v[%4+1], %1\n\tv_mov_b32 v[%4+2], %2\n\tv_mov_b32 v[%4+3], %3" :: "v"(v.x),"v"(v.y),"v"(v.z),"v"(v.w),"n"(R)); }
template<int E> DEV u32x4 colop(const u32* t){ constexpr int c=E>>1; u32x4 v;
  if constexpr(E&1){ v.x=phi(t[c],t[4+c]); v.y=phi(t[8+c],t[12+c]); v.z=phi(t[16+c],t[20+c]); v.w=phi(t[24+c],t[28+c]); }
  else { v.x=plo(t[c],t[4+c]); v.y=plo(t[8+c],t[12+c]); v.z=plo(t[16+c],t[20+c]); v.w=plo(t[24+c],t[28+c]); } return v; }
template<bool AG,int R,int NKB> DEV void permute(){ if constexpr(NKB>0){ u32 t[32];
  if constexpr(AG){ rdA8<R>(t,0); rdA8<R+8>(t+8,0); rdA8<R+16>(t+16,0); rdA8<R+24>(t+24,0); } else { rdV8<R>(t); rdV8<R+8>(t+8); rdV8<R+16>(t+16); rdV8<R+24>(t+24); }
  wr4<AG,R>(colop<0>(t)); wr4<AG,R+4>(colop<1>(t)); wr4<AG,R+8>(colop<2>(t)); wr4<AG,R+12>(colop<3>(t)); wr4<AG,R+16>(colop<4>(t)); wr4<AG,R+20>(colop<5>(t)); wr4<AG,R+24>(colop<6>(t)); wr4<AG,R+28>(colop<7>(t));
  permute<AG,R+32,NKB-1>(); } }
#define MF8(F) asm volatile("s_nop 1\n\tv_mfma_f32_16x16x32_bf16 %0, %8, " F "[%9:%9+3], %0\n\tv_mfma_f32_16x16x32_bf16 %1, %8, " F "[%9+4:%9+7], %1\n\t" \
  "v_mfma_f32_16x16x32_bf16 %2, %8, " F "[%9+8:%9+11], %2\n\tv_mfma_f32_16x16x32_bf16 %3, %8, " F "[%9+12:%9+15], %3\n\tv_mfma_f32_16x16x32_bf16 %4, %8, " F "[%9+16:%9+19], %4\n\t" \
  "v_mfma_f32_16x16x32_bf16 %5, %8, " F "[%9+20:%9+23], %5\n\tv_mfma_f32_16x16x32_bf16 %6, %8, " F "[%9+24:%9+27], %6\n\tv_mfma_f32_16x16x32_bf16 %7, %8, " F "[%9+28:%9+31], %7\n\ts_nop 7" \
  : "+v"(d[0]),"+v"(d[1]),"+v"(d[2]),"+v"(d[3]),"+v"(d[4]),"+v"(d[5]),"+v"(d[6]),"+v"(d[7]) : "v"(a),"n"(R))
template<bool AG,int R,int NKB> DEV void gemv(f32x4* d,const LDSP u32x4* xa){ if constexpr(NKB>0){ u32x4 a=xa[0]; if constexpr(AG) MF8("a"); else MF8("v"); gemv<AG,R+32,NKB-1>(d,xa+4); } }
DEV void mf2(f32x4& d0,f32x4& d1,u32x4 a,u32x4 b0,u32x4 b1){ asm volatile("s_nop 1\n\tv_mfma_f32_16x16x32_bf16 %0, %2, %3, %0\n\tv_mfma_f32_16x16x32_bf16 %1, %2, %4, %1\n\ts_nop 7" : "+v"(d0),"+v"(d1) : "v"(a),"v"(b0),"v"(b1)); }
DEV void zero8(f32x4* d){
  #pragma unroll
  for(int e=0;e<8;e++) d[e]=f32x4{0.f,0.f,0.f,0.f}; }
DEV f32x4 pick4(const f32x4* d,bool hi){ return f32x4{hi?d[4].x:d[0].x,hi?d[5].x:d[1].x,hi?d[6].x:d[2].x,hi?d[7].x:d[3].x}; }
template<int R> DEV void kb_to_lds(LDSP char* dst,int estride,bool wr){
  u32 t[32]; rdA8<R>(t,0); rdA8<R+8>(t+8,0); rdA8<R+16>(t+16,0); rdA8<R+24>(t+24,0);
  if(wr){ *(LDSP u32x4*)(dst)=colop<0>(t); *(LDSP u32x4*)(dst+estride)=colop<1>(t); asm volatile("":::"memory"); SB; *(LDSP u32x4*)(dst+2*estride)=colop<2>(t); *(LDSP u32x4*)(dst+3*estride)=colop<3>(t);
          asm volatile("":::"memory"); SB; *(LDSP u32x4*)(dst+4*estride)=colop<4>(t); *(LDSP u32x4*)(dst+5*estride)=colop<5>(t); asm volatile("":::"memory"); SB; *(LDSP u32x4*)(dst+6*estride)=colop<6>(t); *(LDSP u32x4*)(dst+7*estride)=colop<7>(t); }
}
DEV void lnorm(const float* x,int lane,LDSP char* dst){
  bool on=lane<56; float s=0.f;
  #pragma unroll
  for(int i=0;i<8;i++) s+=x[i];
  float mean=wsum(on?s:0.f)*(1.f/448.f); s=0.f;
  #pragma unroll
  for(int i=0;i<8;i++){ float d=x[i]-mean; s+=d*d; }
  float var=wsum(on?s:0.f)*(1.f/448.f);
  float mb=rbf(mean), den=rbf(sqrtf(rbf(rbf(var)+1e-5f))), r[8];
  #pragma unroll
  for(int i=0;i<8;i++) r[i]=rbf(x[i]-mb)/den;
  if(on) *(LDSP u32x4*)(dst+lane*16)=u32x4{pk2(r[0],r[1]),pk2(r[2],r[3]),pk2(r[4],r[5]),pk2(r[6],r[7])};
}
DEV void gather_y(rsrc_t ws,u32 ybase,LDSP char* smem,int b_,int w,float* x,u32& spins){
  int lane=lane_id(), lc=lane<56?lane:55; u32 off=lc*32, so=ybase+w*4*1792; u32x4 v[8]; u32 ok;
  do{ ok=~0u;
    #pragma unroll
    for(int i=0;i<8;i++){ v[i]=ld16(ws,off+(i&1)*16,so+(i>>1)*1792); ok&=and4(v[i]); }
  } while(retry(ok,spins));
  f32x4 a=__builtin_bit_cast(f32x4,v[0])+__builtin_bit_cast(f32x4,v[2])+__builtin_bit_cast(f32x4,v[4])+__builtin_bit_cast(f32x4,v[6]);
  f32x4 b=__builtin_bit_cast(f32x4,v[1])+__builtin_bit_cast(f32x4,v[3])+__builtin_bit_cast(f32x4,v[5])+__builtin_bit_cast(f32x4,v[7]);
  LDSP f32x4* r=(LDSP f32x4*)(smem+L_RED)+lc*2;
  if(lane<56){ r[w*112]=a; r[w*112+1]=b; }
  LDSP f32x4* xr=(LDSP f32x4*)(smem+L_XR+b_*1792)+lc*2; f32x4 p=xr[0],q=xr[1];
  __syncthreads();
  a=(r[0]+r[112])+(r[224]+r[336]); b=(r[1]+r[113])+(r[225]+r[337]);
  #pragma unroll
  for(int i=0;i<4;i++){ x[i]=rbf(p[i]+rbf(a[i])); x[4+i]=rbf(q[i]+rbf(b[i])); }
  if(w==0&&lane<56){ xr[0]=f32x4{x[0],x[1],x[2],x[3]}; xr[1]=f32x4{x[4],x[5],x[6],x[7]}; }
}
DEV u32x4 q_lds(LDSP char* smem,int b){ int l=lane_id()&31; const LDSP f32x4* r=(const LDSP f32x4*)(smem+L_QR+(l>>4)*2048+b*512)+(l&15)*2;
  f32x4 y=r[0]+r[64], z=r[1]+r[65]; return u32x4{pk2(y.x,y.y),pk2(y.z,y.w),pk2(z.x,z.y),pk2(z.z,z.w)}; }
DEV void own_tile(const Args& A,rsrc_t ws,u32x4 q,int s,int h,int w){
  St st; st_reset(st); pin(st); u32 pp[4]; u32x4 r[8];
  #pragma nounroll
  for(int tl=0;tl<2;tl++){
  const int lane=lane_id(); const u32 vo=lane*16; size_t off=(((size_t)(s&1)*16+h)*16384+16128+w*32+tl*16)*512+4096;
  const char *kp=A.K+off, *vp=A.V+off; asm volatile("" : "+s"(kp),"+s"(vp));
  #pragma unroll
  for(int i=0;i<8;i++) r[i]=__builtin_nontemporal_load((const __attribute__((address_space(1))) u32x4*)(kp+vo+(i*1024-4096)));
  float a[4];
  #pragma unroll
  for(int i=0;i<4;i++) a[i]=pinf(swp16(kdot(r[2*i].x,r[2*i].y,r[2*i].z,r[2*i].w,q),kdot(r[2*i+1].x,r[2*i+1].y,r[2*i+1].z,r[2*i+1].w,q)));
  asm volatile("":::"memory");
  #pragma unroll
  for(int i=0;i<8;i++) r[i]=__builtin_nontemporal_load((const __attribute__((address_space(1))) u32x4*)(vp+vo+(i*1024-4096)));
  spread(soft(a[0],a[1],a[2],a[3],lane,st),pp);
  #pragma unroll
  for(int j=0;j<4;j++){ u32 t[8]={r[j].x,r[j].y,r[j].z,r[j].w,r[j+4].x,r[j+4].y,r[j+4].z,r[j+4].w}; pvr(t,t+4,pp[j],st); }
  }
  publish(ws,st,s,h,56+w);
}
#define FRESH do{ lane=lane_id(); h=h0; w=w0; asm volatile("" : "+s"(h),"+s"(w)); }while(0)
#define g (lane>>4)
#define n (lane&15)
#define n14 (n<14?n:13)
#define tid (w*64+lane)
#define xn (smem+L_XN+w*1792)
DEV void put_y(rsrc_t ws,const f32x4* d,int k,int h,int w,int lane){ f32x4 y=pick4(d,g&1); if(g<2&&n<14) st16(ws,(n*8+g*4)*4,tag4(y.x,y.y,y.z,y.w),WS_Y+(k*16+h)*1792+w*448); }
DEV void chain_init(const Args& A,rsrc_t ws,LDSP char* smem,int h,int w,int odd,u32x4* xv){
  int lane=lane_id(), lc=lane<56?lane:55; xv[0]=*(const u32x4*)(A.x+lc*8); xv[1]=*(const u32x4*)(A.x+448+lc*8); asm volatile("" : "+v"(xv[0]),"+v"(xv[1]));
  zero_slots(ws,h,56+odd*4+w);
  if(tid<112){ u32 z0; asm volatile("v_mov_b32 %0, 0" : "=v"(z0));
    #pragma unroll
    for(int i=0;i<4;i++) st16(ws,tid*16,u32x4{z0,z0,z0,z0},WS_Y+((i*2+odd)*16+h)*1792); }
}
DEV void x_init(const u32x4* xv,LDSP char* smem,int w,bool ln){ int lane=lane_id();
  #pragma unroll
  for(int b=0;b<2;b++){ u32x4 v=xv[b]; float x[8];
    #pragma unroll
    for(int i=0;i<4;i++){ x[2*i]=__uint_as_float(v[i]<<16); x[2*i+1]=__uint_as_float(v[i]&0xffff0000u); }
    if(ln) lnorm(x,lane,xn+b*896);
    if(w==0&&lane<56){ LDSP f32x4* r=(LDSP f32x4*)(smem+L_XR+b*1792)+lane*2; r[0]=f32x4{x[0],x[1],x[2],x[3]}; r[1]=f32x4{x[4],x[5],x[6],x[7]}; } } }
DEV void wait_zeroed(rsrc_t ws,int h,u32 tag,u32& spins){ int lane=lane_id(); u32x4 a; u32x2 z;
  do{ a=ld16(ws,(lane>>2)*512+448+(lane&3)*16,WS_Z); z=__builtin_amdgcn_raw_buffer_load_b64(ws,lane*8,WS_Z+h*512,16); } while(retry(a.x==tag&&a.y==~tag&&a.z==tag&&a.w==~tag&&z.x==tag&&z.y==~tag,spins)); }
DEV void chainA(const Args& A,rsrc_t ws,LDSP char* smem,int lane,int w){
  int h=((blockIdx.x&7)<<1)|(blockIdx.x>>3); const int h0=h, w0=w; u32 spins=0; f32x4 d[8]; TS(0);
  const u32 tag=launch_tag(A.ws);
  { u32x4 xv[2]; chain_init(A,ws,smem,h,w,0,xv);
    const char* pq=(const char*)A.wq+((size_t)(w&1)*224*4096+h*256+(w>>1)*128)*2; const u32 voq=(g*8*4096+n*8)*2;
    ldwn<1,0,8>(voq,pq+(size_t)192*8192,8192); ldwn<0,64,48>(voq,pq,8192); x_init(xv,smem,w,true); }
  FRESH; const char* po=(const char*)A.wo+((size_t)h*256*448+w*112)*2;
  waitv<48>(); kb_to_lds<0>(smem+L_WQ+w*8192+lane*16,1024,true); FRESH;
  ldwn<1,0,32>((g*8*448+n14*8)*2,po,896); waitv<32>(); mark_zeroed(ws,h,56+w,tag); permute<false,64,6>();
  #pragma nounroll
  for(int s=-1;s<4;s++){
    float x[8]; const int b=s&1; __syncthreads(); FRESH;
    if(s>=0){
      TS(4+s*6);
      {
        LDSP f32x4* r=(LDSP f32x4*)(smem+L_RED2)+lane; LDSP float* mv=(LDSP float*)(smem+L_OV);
        int todo=3, hf=0;
        #pragma nounroll
        while(todo){ u32x4 v[8]; const u32 sl=(s*16+h)*64+w*16+hf*8;
          u32x2 ml=__builtin_amdgcn_raw_buffer_load_b64(ws,(lane&7)*8,WS_ML+sl*8,16); u32 ok=ml.x&ml.y;
          #pragma unroll
          for(int i=0;i<8;i++){ v[i]=ld16(ws,lane*16+(i&3)*1024,WS_P+sl*1024+(i>>2)*4096); ok&=and4(v[i]); }
          if(!retry(ok,spins)){
            float m=__uint_as_float(ml.x), M=fmaxf(m,dpp<0xb1>(m)); M=fmaxf(M,dpp<0x4e>(M)); M=fmaxf(M,dpp<0x141>(M));
            float f=__builtin_amdgcn_exp2f(m-M), l=f*__uint_as_float(ml.y); l+=dpp<0xb1>(l); l+=dpp<0x4e>(l); l+=dpp<0x141>(l);
            f32x4 o={0.f,0.f,0.f,0.f};
            #pragma unroll
            for(int i=0;i<8;i++) o+=__builtin_bit_cast(f32x4,v[i])*__int_as_float(__builtin_amdgcn_readlane(__float_as_int(f),i));
            r[w*64+hf*((L_RED3-L_RED2)/16)]=o; mv[w*2+hf]=M; mv[8+w*2+hf]=l; todo&=~(1<<hf);
          }
          if(todo&(1<<(hf^1))) hf^=1;
        }
        TS(5+s*6);
        __syncthreads();
        float m=mv[lane&7], M=fmaxf(m,dpp<0xb1>(m)); M=fmaxf(M,dpp<0x4e>(M)); M=fmaxf(M,dpp<0x141>(M));
        float f=__builtin_amdgcn_exp2f(m-M), l=f*mv[8+(lane&7)]; l+=dpp<0xb1>(l); l+=dpp<0x4e>(l); l+=dpp<0x141>(l); f=f/l;
        f32x4 o={0.f,0.f,0.f,0.f};
        #pragma unroll
        for(int i=0;i<8;i++) o+=r[(i>>1)*64+(i&1)*((L_RED3-L_RED2)/16)]*__int_as_float(__builtin_amdgcn_readlane(__float_as_int(f),i));
        *(LDSP u32x2*)(xn+(lane&31)*16+(lane>>5)*8)=u32x2{pk2(o.x,o.y),pk2(o.z,o.w)};
      }
      FRESH; zero8(d); gemv<true,0,8>(d,(const LDSP u32x4*)xn+g); put_y(ws,d,s*2,h,w,lane);
      TS(6+s*6);
      if(s<2||h==0){
        gather_y(ws,WS_Y+(s*2)*16*1792,smem,b,w,x,spins); __syncthreads();
        gather_y(ws,WS_Y+(s*2+1)*16*1792,smem,b,w,x,spins); FRESH;
        if(s>=2&&w==0&&lane<56) *(u32x4*)(A.out+b*448+lane*8)=u32x4{pk2(x[0],x[1]),pk2(x[2],x[3]),pk2(x[4],x[5]),pk2(x[6],x[7])};
        lnorm(x,lane,xn);
      }
    }
    FRESH; TS(28+(s&3)); if(s>=2) continue;
    FRESH; zero8(d);
    { const LDSP u32x4* xa=(const LDSP u32x4*)(xn+((s<0&&n>=8)?896:0))+(w&1)*28+g; u32x4 a=xa[24]; const LDSP u32x4* wl=(const LDSP u32x4*)(smem+L_WQ+w*8192)+lane;
      #pragma unroll
      for(int e=0;e<8;e+=2) mf2(d[e],d[e+1],a,wl[e*64],wl[e*64+64]);
      gemv<false,64,6>(d,xa); }
    { f32x4 y=pick4(d,g&1); *(LDSP f32x4*)(smem+L_QR+w*1024+(g>>1)*512+(n*8+(g&1)*4)*4)=y; }
    __syncthreads();
    if(w==0||(w==1&&s<0)){ u32x4 q=q_lds(smem,w); bool hi=lane>=32; st16(ws,(lane&31)*32+(lane>>5)*16,u32x4{hi?q.z:q.x,tag,hi?q.w:q.y,tag},WS_Q+((s<0?w:s+2)*16+h)*1024); }
    TS(9+s*6); FRESH;
    if(s<0) ldwn<1,128,32>((g*8*448+n14*8)*2,po+(size_t)128*896,896);
    #pragma nounroll
    for(int j=(s<0?0:1);j<2;j++) own_tile(A,ws,q_lds(smem,j),s<0?j:s+2,h,w);
    if(s<0){ wait_zeroed(ws,h,tag,spins); WAITV(0); permute<true,0,8>(); TS(2); }
  }
  FRESH; if(h==0&&tid==0) __builtin_amdgcn_raw_buffer_store_b32(tag,ws,0,WS_EP,16);
}
DEV void chainG(const Args& A,rsrc_t ws,LDSP char* smem,int lane,int w){
  int h=blockIdx.x-16; const int h0=h, w0=w; u32 spins=0; f32x4 d[8]; TS(0);
  const u32 tag=launch_tag(A.ws);
  { u32x4 xv[2]; chain_init(A,ws,smem,h,w,1,xv);
    ldwn<1,0,56>((g*8*1792+n14*8)*2,(const char*)((w>>1)?A.wu:A.wg)+((size_t)(w&1)*224*1792+h*112)*2,3584); x_init(xv,smem,w,false); }
  FRESH; { const char* pd=(const char*)A.wd+((size_t)h*112*448+w*112)*2; ldwn<0,64,24>((g*8*448+n14*8)*2,pd,896); ldwn<0,160,8>(((g&1)*8*448+n14*8)*2,pd+(size_t)96*896,896); }
  waitv<32>(); mark_zeroed(ws,h,60+w,tag); permute<true,0,7>(); WAITV(0); permute<false,64,4>(); TS(2);
  #pragma nounroll
  for(int s=-2;s<4;s++){
    float x[8]; const int b=s&1; __syncthreads(); FRESH;
    if(s>=0){
      gather_y(ws,WS_Y+(s*2)*16*1792,smem,b,w,x,spins);
      TS(7+s*6); FRESH; lnorm(x,lane,xn); FRESH;
      zero8(d); gemv<true,0,7>(d,(const LDSP u32x4*)xn+(w&1)*28+g);
      { f32x4 y=pick4(d,g&1); if(g<2) *(LDSP f32x4*)(smem+L_RED3+w*1024+(n*8+g*4)*4)=y; }
      __syncthreads();
      { const LDSP float* r=(const LDSP float*)(smem+L_RED3)+lane*2; float hv[2];
        #pragma unroll
        for(int i=0;i<2;i++){ float gg=rbf(r[i]+r[256+i]), uu=rbf(r[512+i]+r[768+i]); hv[i]=rbf(1.f/(1.f+__expf(-gg)))*uu; }
        *(LDSP u32*)(smem+L_HV+w*256+lane*4)=lane<56?pk2(hv[0],hv[1]):0u; }
      zero8(d); FRESH; gemv<false,64,4>(d,(const LDSP u32x4*)(smem+L_HV+w*256)+g); put_y(ws,d,s*2+1,h,w,lane);
      TS(8+s*6);
      if(s<2) gather_y(ws,WS_Y+(s*2+1)*16*1792,smem,b,w,x,spins);
    }
    FRESH;
    if(s<2){ u32x4 q; pollq(ws,WS_Q+((s+2)*16+h)*1024,tag,q,spins); own_tile(A,ws,q,s+2,h,4+w); }
    if(s==-1) wait_zeroed(ws,h,tag,spins);
  }
}
#undef g
#undef n
#undef n14
#undef tid
#undef xn
__global__ __attribute__((amdgpu_num_vgpr(64))) __launch_bounds__(256,1) void fused(Args A){
  extern __shared__ __attribute__((aligned(16))) char smem_[];
  asm volatile("" ::: "v255","a255");
  LDSP char* smem=(LDSP char*)smem_;
  int w=__builtin_amdgcn_readfirstlane(threadIdx.x>>6); asm volatile("" : "+s"(w));
  rsrc_t ws=__builtin_amdgcn_make_buffer_rsrc(A.ws,0,WS_END+131072,0x00020000);
  if(blockIdx.x<16) chainA(A,ws,smem,lane_id(),w); else if(blockIdx.x<32) chainG(A,ws,smem,lane_id(),w); else streamer(A,ws,smem,lane_id(),w);
}
extern "C" void kernel_launch(void* const* d_in,const int* in_sizes,int n_in,void* d_out,int out_size,void* d_ws,size_t ws_size,hipStream_t stream){
  Args A; A.x=(const u16*)d_in[0]; A.K=(const char*)d_in[1]; A.V=(const char*)d_in[2]; A.wq=(const u16*)d_in[3]; A.wo=(const u16*)d_in[4];
  A.wg=(const u16*)d_in[6]; A.wu=(const u16*)d_in[7]; A.wd=(const u16*)d_in[8]; A.ws=(char*)d_ws; A.out=(u16*)d_out;
  (void)hipFuncSetAttribute((const void*)fused,hipFuncAttributeMaxDynamicSharedMemorySize,163840);
  fused<<<256,256,163840,stream>>>(A);
}
```
